```python
import torch

def _softmax(x, axis=-1):
    return torch.softmax(x, dim=axis)
def _gelu(x):
    return torch.nn.functional.gelu(x, approximate="tanh")
def _silu(x):
    return torch.nn.functional.silu(x)
def _sigmoid(x):
    return torch.sigmoid(x)
def _layernorm(x, axis=-1, eps=1e-5):
    m = x.mean(dim=axis, keepdim=True)
    v = x.var(dim=axis, keepdim=True, unbiased=False)
    return (x - m) / torch.sqrt(v + eps)
def _scatter_add(tgt, idx, src, axis=0):
    return tgt.clone().index_add_(axis, idx, src)
def _segment_sum(src, seg, n):
    out = torch.zeros((n,) + src.shape[1:], dtype=src.dtype)
    return out.index_add_(0, seg, src)
def _pad(x, pw):
    flat = tuple(v for pair in reversed(pw) for v in pair)
    return torch.nn.functional.pad(x, flat)

kernel_name = 'parallel_sum__sliding_block__glu_gated_reglu__prompt'

def setup_inputs():

    _w = (lambda a: (lambda t: t.to(torch.bfloat16) if t.is_floating_point() else t)(torch.as_tensor(a)))
    def _A_setup():
        B, S, D = 1, 2048, 640
        return {
            "x": _w((lambda *s: torch.randn(*s, dtype=torch.bfloat16) * 0.05)(B, S, D)),
            "wqkv": _w((lambda *s: torch.randn(*s, dtype=torch.bfloat16) * 0.05)(D, 3 * D)),
            "wo": _w((lambda *s: torch.randn(*s, dtype=torch.bfloat16) * 0.05)(D, D)),
        }
    _A_in = _A_setup()

    def _B_setup():
        B, S, D, F = 1, 2048, 640, 1536
        return {
            "x": _w((lambda *s: torch.randn(*s, dtype=torch.bfloat16) * 0.05)(B, S, D)),
            "W_gate": _w((lambda *s: torch.randn(*s, dtype=torch.bfloat16) * 0.8)(D, F) * (2.0 / D) ** 0.5),
            "W_up": _w((lambda *s: torch.randn(*s, dtype=torch.bfloat16) * 0.8)(D, F) * (2.0 / D) ** 0.5),
            "W_down": _w((lambda *s: torch.randn(*s, dtype=torch.bfloat16) * 0.8)(F, D) * (1.0 / F) ** 0.5),
        }
    _B_in = _B_setup()

    return {
        "A_x": _A_in["x"],
        "A_wqkv": _A_in["wqkv"],
        "A_wo": _A_in["wo"],
        "B_x": _B_in["x"],
        "B_W_gate": _B_in["W_gate"],
        "B_W_up": _B_in["W_up"],
        "B_W_down": _B_in["W_down"],
    }

def reference(A_x, A_wqkv, A_wo, B_x, B_W_gate, B_W_up, B_W_down):
    def _A_ref(x, wqkv, wo):
        B, S, D = x.shape
        H = 32; Dh = D // H; W = 256
        qkv = (lambda x, *d: x.permute(*d))((x @ wqkv).reshape(B, S, 3, H, Dh), 2, 0, 3, 1, 4)
        q, k, v = qkv[0], qkv[1], qkv[2]
        scores = torch.einsum("bhqd,bhkd->bhqk", q, k) * (Dh ** -0.5)
        idx = torch.arange(S)
        mask = (idx[:, None] - idx[None, :] >= 0) & (idx[:, None] - idx[None, :] < W)
        scores = torch.where(mask[None, None], scores, -1e9)
        p = _softmax(scores, axis=-1)
        o = torch.einsum("bhqk,bhkd->bhqd", p, v)
        return (lambda x, *d: x.permute(*d))(o, 0, 2, 1, 3).reshape(B, S, D) @ wo

    def _B_ref(x, W_gate, W_up, W_down):
        gate = torch.einsum("bsd,df->bsf", x, W_gate)
        up = torch.einsum("bsd,df->bsf", x, W_up)
        gate = gate * (lambda x, dt: x.to(dt))(gate > 0, gate.dtype)
        h = gate * up
        return torch.einsum("bsf,fd->bsd", h, W_down)

    x = A_x
    xn = _layernorm(x, axis=-1)
    return x + _A_ref(xn, A_wqkv, A_wo) + _B_ref(xn, B_W_gate, B_W_up, B_W_down)
```

```cpp
#include <hip/hip_runtime.h>
#include <hip/hip_bf16.h>
typedef __hip_bfloat16 bf16;
constexpr int S=2048, D=640, F=1536, H=32, DH=20, W=256;
__device__ inline float b2f(bf16 x){return __bfloat162float(x);}
__device__ inline bf16 f2b(float x){return __float2bfloat16(x);}
__device__ inline float rb(float x){return b2f(f2b(x));}

__global__ void ln_k(const bf16* x, bf16* xn){
  int r=blockIdx.x; int l=threadIdx.x;
  float s=0; for(int i=l;i<D;i+=64) s+=b2f(x[r*D+i]);
  for(int m=32;m;m>>=1) s+=__shfl_xor(s,m);
  float mean=s/D; float v=0;
  for(int i=l;i<D;i+=64){float d=b2f(x[r*D+i])-mean; v+=d*d;}
  for(int m=32;m;m>>=1) v+=__shfl_xor(v,m);
  v/=D;
  float mb=rb(mean), vb=rb(v); float sd=rb(sqrtf(rb(vb+1e-5f)));
  for(int i=l;i<D;i+=64) xn[r*D+i]=f2b(rb(b2f(x[r*D+i])-mb)/sd);
}
__global__ void gemm_k(const bf16* A,const bf16* B,bf16* C,int M,int N,int K){
  __shared__ float sa[16][17], sb[16][17];
  int tx=threadIdx.x&15, ty=threadIdx.x>>4;
  int row=blockIdx.y*16+ty, col=blockIdx.x*16+tx; float acc=0;
  for(int k=0;k<K;k+=16){
    sa[ty][tx]=b2f(A[row*K+k+tx]); sb[ty][tx]=b2f(B[(k+ty)*N+col]);
    __syncthreads();
    for(int j=0;j<16;j++) acc+=sa[ty][j]*sb[j][tx];
    __syncthreads();
  }
  C[row*N+col]=f2b(acc);
}
__global__ void attn_k(const bf16* qkv, bf16* o){
  int q=blockIdx.x, h=blockIdx.y, l=threadIdx.x;
  float qq[DH]; for(int d=0;d<DH;d++) qq[d]=b2f(qkv[q*3*D+h*DH+d]);
  float sc[4]; float mx=-1e30f;
  for(int i=0;i<4;i++){ int k=q-(l+64*i); float s=-1e30f;
    if(k>=0){ s=0; for(int d=0;d<DH;d++) s+=qq[d]*b2f(qkv[k*3*D+D+h*DH+d]); s=rb(rb(s)*rb(0.22360679775f)); }
    sc[i]=s; mx=fmaxf(mx,s);}
  for(int m=32;m;m>>=1) mx=fmaxf(mx,__shfl_xor(mx,m));
  float sum=0; for(int i=0;i<4;i++){ int k=q-(l+64*i); sc[i]= k>=0? __expf(sc[i]-mx):0.f; sum+=sc[i];}
  for(int m=32;m;m>>=1) sum+=__shfl_xor(sum,m);
  for(int d=0;d<DH;d++){ float a=0; for(int i=0;i<4;i++){int k=q-(l+64*i); if(k>=0) a+=rb(sc[i]/sum)*b2f(qkv[k*3*D+2*D+h*DH+d]);}
    for(int m=32;m;m>>=1) a+=__shfl_xor(a,m);
    if(l==0) o[q*D+h*DH+d]=f2b(a);}
}
__global__ void glu_k(const bf16* g,const bf16* u,bf16* h,int n){int i=blockIdx.x*256+threadIdx.x; if(i<n){float a=b2f(g[i]); h[i]=f2b((a>0?a:0.f)*b2f(u[i]));}}
__global__ void fin_k(const bf16* x,const bf16* a,const bf16* b,bf16* o,int n){int i=blockIdx.x*256+threadIdx.x; if(i<n) o[i]=f2b(rb(b2f(x[i])+b2f(a[i]))+b2f(b[i]));}

extern "C" void kernel_launch(void* const* d_in, const int* in_sizes, int n_in, void* d_out, int out_size, void* d_ws, size_t ws_size, hipStream_t st){
  const bf16* x=(const bf16*)d_in[0]; const bf16* wqkv=(const bf16*)d_in[1]; const bf16* wo=(const bf16*)d_in[2];
  const bf16* wg=(const bf16*)d_in[4]; const bf16* wu=(const bf16*)d_in[5]; const bf16* wd=(const bf16*)d_in[6];
  bf16* ws=(bf16*)d_ws; bf16* xn=ws; bf16* qkv=xn+S*D; bf16* o=qkv+S*3*D; bf16* g=o+S*D; bf16* u=g+S*F; bf16* h=u+S*F; bf16* a=h+S*F; bf16* b=a+S*D;
  ln_k<<<S,64,0,st>>>(x,xn);
  gemm_k<<<dim3(3*D/16,S/16),256,0,st>>>(xn,wqkv,qkv,S,3*D,D);
  attn_k<<<dim3(S,H),64,0,st>>>(qkv,o);
  gemm_k<<<dim3(D/16,S/16),256,0,st>>>(o,wo,a,S,D,D);
  gemm_k<<<dim3(F/16,S/16),256,0,st>>>(xn,wg,g,S,F,D);
  gemm_k<<<dim3(F/16,S/16),256,0,st>>>(xn,wu,u,S,F,D);
  glu_k<<<S*F/256,256,0,st>>>(g,u,h,S*F);
  gemm_k<<<dim3(D/16,S/16),256,0,st>>>(h,wd,b,S,D,F);
  fin_k<<<S*D/256,256,0,st>>>(x,a,b,(bf16*)d_out,S*D);
}
```

```cpp
#include <hip/hip_runtime.h>
#include <stdint.h>
#include <type_traits>

#define STAMPS 0
#define FINE 0
#define STAMP_TID 0
#define ABL 0
#define RPSLEEP 0
#define PRO 8
#define NLAUNCH 1

typedef __bf16 bf16x8 __attribute__((ext_vector_type(8)));
typedef __bf16 bf16x2 __attribute__((ext_vector_type(2)));
typedef short s16x4 __attribute__((ext_vector_type(4)));
typedef float f32x16 __attribute__((ext_vector_type(16)));
typedef float f32x4 __attribute__((ext_vector_type(4)));
typedef float f32x2 __attribute__((ext_vector_type(2)));
typedef unsigned u32x4 __attribute__((ext_vector_type(4)));
typedef unsigned u32x2 __attribute__((ext_vector_type(2)));
typedef unsigned long long u64;
#define LAS __attribute__((address_space(3)))
#define GAS __attribute__((address_space(1)))
#define DEV __device__ __forceinline__
template <int V> using IC = std::integral_constant<int, V>;

constexpr int SEQ = 2048, DM = 640, FF = 1536;
constexpr unsigned QKV_TB = 240 * 1024;
constexpr unsigned QKV_BYTES = 32 * QKV_TB;
constexpr unsigned A2_MT = 272 * 1024;
constexpr unsigned A2_BYTES = 32 * A2_MT;
constexpr unsigned R_J = 68 * 5 * 1024;
constexpr unsigned R_BYTES = 8 * R_J;
constexpr unsigned WS_FLAGS = 0, WS_STAT = 16384, WS_QKV = 65536, WS_A2 = WS_QKV + QKV_BYTES, WS_R = WS_A2 + A2_BYTES, WS_STAMP = WS_R + R_BYTES;
constexpr unsigned FL_EPOCH = 0, FL_1 = 2048, FL_2 = 4096, FL_3 = 8192;

constexpr unsigned UNIT = 16640;
constexpr unsigned upos(int k) { return ((k + 8) % 9) * UNIT; }
constexpr unsigned AROW = 8256;
constexpr unsigned L_FR = 133248, L_Q = L_FR + 8192;
constexpr unsigned L_M = L_Q + 16384;
constexpr unsigned L_E = 160768, L_ST = L_E + 2048;
static_assert(L_FR % 256 == 128 && L_FR >= 8 * UNIT && L_M + 2048 <= L_E && L_E >= 9 * UNIT, "attention image");
constexpr unsigned L_A0 = 6 * AROW;
static_assert(L_A0 + 5 * 16384 <= L_FR, "phase-2 image");
constexpr unsigned a_slot(int r) { return (r % 9) < 6 ? L_A0 + (r % 9) * 16384 : ((r % 9) - 6) * 16384; }
constexpr unsigned PROW = 336, PCLS = 64 * PROW;

template <int I, int N, class F> DEV void static_for(F&& f) { if constexpr (I < N) { f(IC<I>{}); static_for<I + 1, N>(f); } }

DEV u32x4 make_srd(const void* p, unsigned bytes) { u64 a = (u64)p; return (u32x4){(unsigned)a, (unsigned)(a >> 32) & 0xffffu, bytes, 0x00020000u}; }
template <unsigned LOFF, unsigned SOFF> DEV void dma16(unsigned lb, unsigned voff, u32x4 srd, unsigned sb) {
  unsigned t;
  asm volatile("s_add_u32 m0, %1, %5\n\ts_add_u32 %0, %4, %6\n\tbuffer_load_dwordx4 %2, %3, %0 offen lds"
               : "=&s"(t) : "s"(lb), "v"(voff), "s"(srd), "s"(sb), "i"(LOFF), "i"(SOFF) : "memory", "scc");
}
template <unsigned LOFF, unsigned SOFF> DEV void dma16c(unsigned lb, unsigned voff, u32x4 srd, unsigned sb) {
  unsigned t;
  asm volatile("s_add_u32 m0, %1, %5\n\ts_add_u32 %0, %4, %6\n\tbuffer_load_dwordx4 %2, %3, %0 offen sc1 lds"
               : "=&s"(t) : "s"(lb), "v"(voff), "s"(srd), "s"(sb), "i"(LOFF), "i"(SOFF) : "memory", "scc");
}
template <unsigned LOFF, unsigned SOFF> DEV void dma16c_x4(unsigned lb, unsigned voff, u32x4 srd, unsigned sb) {
  unsigned t;
  asm volatile("s_add_u32 m0, %1, %5\n\ts_add_u32 %0, %4, %6\n\tbuffer_load_dwordx4 %2, %3, %0 offen sc1 lds\n\t"
               "buffer_load_dwordx4 %2, %3, %0 offen offset:1024 sc1 lds\n\tbuffer_load_dwordx4 %2, %3, %0 offen offset:2048 sc1 lds\n\t"
               "buffer_load_dwordx4 %2, %3, %0 offen offset:3072 sc1 lds"
               : "=&s"(t) : "s"(lb), "v"(voff), "s"(srd), "s"(sb), "i"(LOFF), "i"(SOFF) : "memory", "scc");
}
template <unsigned SOFF> DEV void ld16cs(u32x4& v, unsigned voff, u32x4 srd, unsigned sb) {
  unsigned t;
  asm volatile("s_add_u32 %1, %4, %5\n\tbuffer_load_dwordx4 %0, %2, %3, %1 offen sc1" : "=v"(v), "=&s"(t) : "v"(voff), "s"(srd), "s"(sb), "i"(SOFF) : "memory", "scc");
}
template <int OFF> DEV void st16c(u32x4 v, unsigned voff, u32x4 srd, unsigned soff) {
  asm volatile("buffer_store_dwordx4 %0, %1, %2, %3 offen offset:%4 sc1\n\ts_nop 1" ::"v"(v), "v"(voff), "s"(srd), "s"(soff), "i"(OFF) : "memory");
}
template <int OFF> DEV void st8c(u32x2 v, unsigned voff, u32x4 srd, unsigned soff) {
  asm volatile("buffer_store_dwordx2 %0, %1, %2, %3 offen offset:%4 sc1\n\ts_nop 1" ::"v"(v), "v"(voff), "s"(srd), "s"(soff), "i"(OFF) : "memory");
}
template <int OFF> DEV void ld16c(u32x4& v, unsigned voff, u32x4 srd, unsigned soff) {
  asm volatile("buffer_load_dwordx4 %0, %1, %2, %3 offen offset:%4 sc1" : "=v"(v) : "v"(voff), "s"(srd), "s"(soff), "i"(OFF) : "memory");
}
template <int OFF> DEV void ld16(u32x4& v, unsigned voff, u32x4 srd, unsigned soff) {
  asm volatile("buffer_load_dwordx4 %0, %1, %2, %3 offen offset:%4" : "=v"(v) : "v"(voff), "s"(srd), "s"(soff), "i"(OFF) : "memory");
}
template <int N> DEV void wait_vm() { asm volatile("s_waitcnt vmcnt(%0)" ::"i"(N > 63 ? 63 : N) : "memory"); }
template <int N> DEV void wait_bar() { asm volatile("s_waitcnt vmcnt(%0) lgkmcnt(0)\n\ts_barrier" ::"i"(N > 63 ? 63 : N) : "memory"); }
DEV void bar() { asm volatile("s_barrier" ::: "memory"); }
DEV void vm0_bar() { asm volatile("s_waitcnt vmcnt(0)\n\ts_barrier" ::: "memory"); }
DEV void lgkm_bar() { asm volatile("s_waitcnt lgkmcnt(0)\n\ts_barrier" ::: "memory"); }
#define PIN(x) asm volatile("" : "+v"(x))
#define SBAR() __builtin_amdgcn_sched_barrier(0)
DEV int fresh_lane() { int l; asm volatile("v_mbcnt_lo_u32_b32 %0, -1, 0\n\tv_mbcnt_hi_u32_b32 %0, -1, %0" : "=v"(l)); return l; }

DEV bf16x8 lds_b128(unsigned a) { return *(const LAS bf16x8*)a; }
DEV s16x4 lds_tr(unsigned a) { return __builtin_amdgcn_ds_read_tr16_b64_v4i16((LAS s16x4*)a); }
DEV bf16x8 join(s16x4 lo, s16x4 hi) { return __builtin_bit_cast(bf16x8, __builtin_shufflevector(lo, hi, 0, 1, 2, 3, 4, 5, 6, 7)); }
DEV bf16x8 asbf(u32x4 v) { return __builtin_bit_cast(bf16x8, v); }
DEV f32x16 mfma32(bf16x8 a, bf16x8 b, f32x16 c) { return __builtin_amdgcn_mfma_f32_32x32x16_bf16(a, b, c, 0, 0, 0); }
DEV f32x4 mfma16(bf16x8 a, bf16x8 b, f32x4 c) { return __builtin_amdgcn_mfma_f32_16x16x32_bf16(a, b, c, 0, 0, 0); }
DEV unsigned pk2(float lo, float hi) { return __builtin_bit_cast(unsigned, (bf16x2){(__bf16)lo, (__bf16)hi}); }
DEV float rbf(float x) { return (float)(__bf16)x; }
DEV float max3(float a, float b, float c) { float r; asm("v_max3_f32 %0, %1, %2, %3" : "=v"(r) : "v"(a), "v"(b), "v"(c)); return r; }
DEV float relu(float x) { const int i = __float_as_int(x); return __int_as_float(i > 0 ? i : 0); }
DEV u32x2 split3(float c) { const float a = rbf(c), b = rbf(c - a), d = c - a - b; return (u32x2){pk2(a, b), pk2(d, 0.f)}; }

struct Params {
  const void *x, *wqkv, *wo, *wg, *wu, *wd;
  void* out;
  char* ws;
};

#define STAMP(i) do {} while (0)
#define LSTAMP(i) do {} while (0)

constexpr unsigned SPIN_CAP = 200000;
template <int NF> DEV void poll_flags(const char* fl, u64 tag, int lane) {
#pragma unroll 1
  for (unsigned spin = 0; spin < SPIN_CAP; ++spin) {
    bool ok = true;
#pragma unroll
    for (int i = 0; i < (NF + 63) / 64; ++i) {
      const int idx = (NF >= 64) ? lane + 64 * i : (lane & (NF - 1));
      ok &= __hip_atomic_load((const GAS u64*)fl + idx, __ATOMIC_RELAXED, __HIP_MEMORY_SCOPE_AGENT) == tag;
    }
    if (__all(ok)) break;
    __builtin_amdgcn_s_sleep(1);
  }
  asm volatile("" ::: "memory");
}
DEV void put_flag(char* fl, int idx, u64 tag) { __hip_atomic_store((GAS u64*)fl + idx, tag, __ATOMIC_RELAXED, __HIP_MEMORY_SCOPE_AGENT); }

constexpr int NBR = 6;
constexpr int imin(int a, int b) { return a < b ? a : b; }
constexpr int fill_end(int I) { return I < 0 ? PRO : imin(imin(fill_end(I - 1) + 2, 36), I <= 10 ? 2 * I + 8 : I + 19); }
constexpr int fill_beg(int I) { return fill_end(I - 1); }
constexpr int I_ST = 7, I_RP = 10, I_KQ = 20, I_V = 21;
constexpr int extra_ops(int I) { return I == I_ST ? 1 : (I == I_RP ? 4 : (I == I_KQ ? 9 : (I == I_V ? 6 : 0))); }
constexpr int bl_end(int I) { return I < 22 ? 0 : 5 * (imin(I, 23) - 21); }
constexpr int ops_in(int I) { return 4 * (fill_end(I) - fill_beg(I)) + extra_ops(I) + bl_end(I) - bl_end(I - 1); }
constexpr int ops_between(int I0, int I1) { int n = 0; for (int i = I0; i < I1; ++i) n += ops_in(i); return n; }
constexpr int younger_unit(int k, int J) {
  if (k < PRO) return 4 * (PRO - 1 - k) + ops_between(0, J);
  int I = 0; while (fill_end(I) <= k) ++I;
  return ops_in(I) - 4 * (k + 1 - fill_beg(I)) + ops_between(I + 1, J);
}
template <int CTRL> DEV float dpp_add(float x) {
  return x + __int_as_float(__builtin_amdgcn_update_dpp(0, __float_as_int(x), CTRL, 0xf, 0xf, true));
}
DEV float swap_add32(float a, float b) { auto r = __builtin_amdgcn_permlane32_swap(__float_as_uint(a), __float_as_uint(b), false, false); return __uint_as_float(r[0]) + __uint_as_float(r[1]); }
DEV float swap_add16(float a, float b) { auto r = __builtin_amdgcn_permlane16_swap(__float_as_uint(a), __float_as_uint(b), false, false); return __uint_as_float(r[0]) + __uint_as_float(r[1]); }
DEV u64 uni64(u32x2 v) { return ((u64)(unsigned)__builtin_amdgcn_readfirstlane(v[1]) << 32) | (unsigned)__builtin_amdgcn_readfirstlane(v[0]); }

__global__ __launch_bounds__(512, 2) void fused(Params P) {
  __shared__ __attribute__((aligned(1024))) char smem[163840];
  const unsigned L0 = (unsigned)(uintptr_t)(LAS char*)smem;
  const int w = __builtin_amdgcn_readfirstlane(threadIdx.x >> 6);
  const int b = blockIdx.x, xcd = b & 7, rr = b >> 3;
  const u32x4 srd_qkv = make_srd(P.ws + WS_QKV, QKV_BYTES);
  const u32x4 srd_a2 = make_srd(P.ws + WS_A2, A2_BYTES);
  const u32x4 srd_r = make_srd(P.ws + WS_R, R_BYTES);
  const u32x4 srd_x = make_srd(P.x, SEQ * DM * 2);
  const u32x4 srd_st = make_srd(P.ws + WS_STAT, SEQ * 16);
  const int p = (xcd >> 2) * 8 + (rr & 7), c = (xcd & 3) * 4 + (rr >> 3);
  const int tyB = c & 1, ci = c >> 1;
  const int ub = tyB ? 24 + ci : 3 * ci + 2;
  const int pg = tyB ? 8 + ci : 16 + ci;
  const int head = 4 * xcd + (rr & 3), qb = rr >> 2, par = head & 1, o0 = (5 * head) >> 1;
  const int mt = 4 * xcd + (rr & 3), jn = rr >> 2;
  u32x4 xres[2];

  if (w < 4) {
    u64 tag;
    {
      __builtin_amdgcn_s_setprio(3);
      const int lane = fresh_lane(), l31 = lane & 31;
      const int row = p * 128 + 8 * c + 2 * w;
      u32x2 ep;
      u32x4 xv[3];
      const unsigned vo = (lane >> 5) * 1280 + l31 * 16;
      ld16<0>(xv[0], vo, srd_x, row * 1280);
      ld16<512>(xv[1], vo, srd_x, row * 1280);
      ld16<1024>(xv[2], l31 < 16 ? vo : 0x7ff00000u, srd_x, row * 1280);
      asm volatile("global_load_dwordx2 %0, %1, %2 sc1" : "=v"(ep) : "v"(0), "s"(P.ws + WS_FLAGS + FL_EPOCH) : "memory");
      if (w == 0) {
        unsigned c0, c1, len, dummy;
        asm volatile("s_getpc_b64 s[98:99]\n.Lpc_here:\n\ts_sub_u32 %0, s98, .Lpc_here-_Z5fused6Params\n\ts_subb_u32 %1, s99, 0\n\ts_mov_b32 %2, .Lcode_end-_Z5fused6Params"
                     : "=s"(c0), "=s"(c1), "=s"(len) :: "s98", "s99", "scc");
        const u32x4 srd_c = {c0, c1 & 0xffffu, len, 0x00020000u};
        asm volatile("buffer_load_dword %0, %1, %2, 0 offen" : "=v"(dummy) : "v"(lane < 16 ? (lane * 32 + rr) * 128 : 0x7ff00000u), "s"(srd_c) : "memory");
        wait_vm<0>();
        asm volatile("" :: "v"(dummy));
      }
      __builtin_amdgcn_s_setprio(0);
      STAMP(56);
      wait_vm<0>();
      PIN(ep); PIN(xv[0]); PIN(xv[1]); PIN(xv[2]);
      STAMP(57);
      tag = uni64(ep) + 1;
      float s1 = 0.f, s2 = 0.f;
      const bf16x2 one2 = __builtin_bit_cast(bf16x2, 0x3f803f80u);
#pragma unroll
      for (int i = 0; i < 12; ++i) {
        const unsigned raw = xv[i >> 2][i & 3];
        const bf16x2 v = __builtin_bit_cast(bf16x2, raw);
        s1 = __builtin_amdgcn_fdot2_f32_bf16(v, one2, s1, false);
        s2 = __builtin_amdgcn_fdot2_f32_bf16(v, v, s2, false);
      }
      s1 = dpp_add<0xb1>(s1); s2 = dpp_add<0xb1>(s2);
      s1 = dpp_add<0x4e>(s1); s2 = dpp_add<0x4e>(s2);
      s1 = dpp_add<0x141>(s1); s2 = dpp_add<0x141>(s2);
      s1 = dpp_add<0x140>(s1); s2 = dpp_add<0x140>(s2);
      s1 = swap_add16(s1, s1); s2 = swap_add16(s2, s2);
      const float mean = s1 * (1.f / DM), var = fmaxf(s2 * (1.f / DM) - mean * mean, 0.f);
      const unsigned pay = pk2(-mean, __builtin_amdgcn_sqrtf(rbf(rbf(var) + 1e-5f)));
      const u32x2 gr = {((unsigned)tag << 16) | ((lane & 1) ? pay >> 16 : pay & 0xffffu), (unsigned)(tag >> 16)};
      st8c<0>(gr, l31 < 2 ? (lane >> 5) * 16 + l31 * 8 : 0x7ff00000u, srd_st, row * 16);
    }
    STAMP(58);
    {
      constexpr int D = 5;
      bf16x8 xf[40], wf[D];
      f32x16 acc[4];
      unsigned ax, aw;
      {
        const int lane = fresh_lane(), l31 = lane & 31, hh = lane >> 5;
        ax = L0 + (l31 >> 1) * 1040 + (2 * w + (l31 & 1)) * 128 + hh * 16;
      }
      auto set_aw = [&](bool beta) {
        const int lane = fresh_lane(), hh = lane >> 5, blk = (lane >> 4) & 1, q = (lane & 15) >> 2, pp = lane & 3;
        const int pi = ((pp & 1) << 1) | (pp >> 1);
        aw = L0 + (q & 1) * 128 + hh * 512 + 8 * pi + (beta ? (q >> 1) * 8256 + 32 * blk : (2 * blk + (q >> 1)) * 4128);
      };
      set_aw(false);
      unsigned cur;
      u32x2 mf;
      float rstd;
      constexpr auto z2f = [](int z) constexpr { return z < 160 ? z : (z < 164 ? -1 : (z < 244 ? z - 4 : (z < 248 ? -1 : (z < 408 ? z - 8 : -1)))); };
      constexpr auto zct = [](int z) constexpr { return z < 164 ? z - 160 : (z < 248 ? z - 244 : z - 408); };
      auto rd = [&](auto Z_) {
        constexpr int z = Z_, f = z2f(z);
        if constexpr (f < 0) {
          constexpr int t = zct(z);
          if constexpr (t == 0) {
            bar();
            if constexpr (FINE == 1) LSTAMP(z == 160 ? 17 : (z == 244 ? 22 : 32));
            const int lane = fresh_lane();
            cur = L0 + L_E + lane * 8; PIN(cur);
            mf = *(const LAS u32x2*)(L0 + L_ST + w * 256 + (lane & 31) * 8);
          }
          const u32x2 e = *(const LAS u32x2*)(cur + t * 512);
          wf[z % D] = asbf((u32x4){e[0], e[1], e[0], e[1]});
        } else {
          constexpr int wu = f >> 4, i = f & 15, k = wu < 10 ? 2 * wu + 1 : wu + 11;
          if constexpr (i == 0) {
            if constexpr (wu == 17) vm0_bar(); else if constexpr (wu != 10 && wu != 15) bar();
            if constexpr (FINE == 1 && wu != 10 && wu != 15) LSTAMP(wu ? 7 + wu : 63);
            if constexpr (wu == 10) set_aw(true);
            if constexpr (wu == 15) set_aw(false);
            cur = aw + upos(k); PIN(cur);
            if constexpr (wu < 10) {
              unsigned cx = ax + upos(k - 1); PIN(cx);
#pragma unroll
              for (int s = 0; s < 4; ++s) xf[4 * wu + s] = lds_b128(cx + 32 * s);
            }
          }
          constexpr bool beta = wu >= 10 && wu < 15;
          constexpr int off = beta ? (i >> 1) * 1024 + (i & 1) * 64 : (i >> 2) * 1024 + (i & 3) * 32;
          wf[z % D] = join(lds_tr(cur + off), lds_tr(cur + off + 256));
        }
      };
      auto epi = [&](auto PASS_) {
        constexpr int pass = PASS_, nt = pass == 1 ? 2 : 4;
        __builtin_amdgcn_s_setprio(3);
        const int lane = fresh_lane();
        const unsigned vo = (lane >> 5) * 1024 + (32 * (w & 1) + (lane & 31)) * 16;
        const int tb = 2 * p + (w >> 1);
        constexpr float QS = 0.22360679774997896f * 1.4426950408889634f;
        if (pass == 2 || (pass == 0 && tyB)) {
          const float sc = rstd * rstd;
          const unsigned so = tb * A2_MT + (pass == 2 ? pg : ci) * 8192;
#pragma unroll
          for (int t = 0; t < nt; ++t) {
            u32x4 v;
#pragma unroll
            for (int r = 0; r < 4; ++r) {
              const f32x2 g = {relu(acc[t][2 * r]), relu(acc[t][2 * r + 1])}, u = {acc[t][2 * r + 8], acc[t][2 * r + 9]};
              const f32x2 h = g * (u * sc);
              v[r] = pk2(h[0], h[1]);
            }
            st16c<0>(v, vo, srd_a2, so + t * 2048);
            SBAR();
          }
        } else {
          const int u0 = pass == 1 ? ub : 3 * ci, u1 = pass == 1 ? ub : 3 * ci + 1;
          const float s0 = rstd * (u0 < 10 ? QS : 1.f), s1 = rstd * (u1 < 10 ? QS : 1.f);
          const unsigned vq = u0 < 30 ? vo : 0x7ff00000u;
          const unsigned so = tb * QKV_TB + (u0 < 30 ? u0 : 0) * 8192;
#pragma unroll
          for (int t = 0; t < nt; ++t) {
#pragma unroll
            for (int hf = 0; hf < 2; ++hf) {
              u32x4 v;
#pragma unroll
              for (int r = 0; r < 4; ++r) {
                const f32x2 h = (f32x2){acc[t][8 * hf + 2 * r], acc[t][8 * hf + 2 * r + 1]} * (hf ? s1 : s0);
                v[r] = pk2(h[0], h[1]);
              }
              st16c<0>(v, vq, srd_qkv, so + (pass == 1 ? t * 4096 + hf * 2048 : t * 2048 + hf * 8192));
              SBAR();
            }
          }
        }
        __builtin_amdgcn_s_setprio(0);
      };
      static_for<0, D - 1>(rd);
      STAMP(1);
      static_for<0, 412>([&](auto Z_) {
        constexpr int z = Z_, f = z2f(z);
        if constexpr (z + D - 1 < 412) rd(IC<z + D - 1>{});
        SBAR();
        if constexpr (f < 0) {
          constexpr int t = (z > 200 && z < 300) ? zct(z) & 1 : zct(z);
          if constexpr (zct(z) == 0) { rstd = __uint_as_float(mf[1]); mf[1] = mf[0] & 0xffffu; }
          const u32x4 e = __builtin_bit_cast(u32x4, wf[z % D]);
          acc[t] = __builtin_amdgcn_mfma_f32_32x32x8bf16_1k(__builtin_bit_cast(s16x4, (u32x2){e[0], e[1]}), __builtin_bit_cast(s16x4, mf), acc[t], 0, 0, 0);
        } else {
          constexpr int wu = f >> 4, i = f & 15;
          constexpr bool beta = wu >= 10 && wu < 15;
          constexpr int t = beta ? (i & 1) : (i & 3);
          constexpr int xi = wu < 10 ? 4 * wu + (i >> 2) : (beta ? 8 * (wu - 10) + (i >> 1) : 4 * (wu - 15) + (i >> 2));
          if constexpr (xi == 0) acc[t] = mfma32(wf[z % D], xf[xi], (f32x16){});
          else acc[t] = mfma32(wf[z % D], xf[xi], acc[t]);
        }
        SBAR();
        if constexpr (z == 163) { epi(IC<0>{}); STAMP(2); SBAR(); }
        if constexpr (z == 247) { epi(IC<1>{}); STAMP(3); SBAR(); }
        if constexpr (z == 411) { epi(IC<2>{}); STAMP(4); SBAR(); }
      });
    }
    bar();
    STAMP(5);
    const int lane = fresh_lane();
    {
      const int l31 = lane & 31, hh = lane >> 5, blk = (lane >> 4) & 1, q4 = (lane & 15) >> 2, pp = lane & 3;
      const unsigned tk = L0 + (64 * w + l31) * 16;
      const unsigned ak0 = tk + hh * AROW, ak1 = tk + (hh ? L_FR : 2 * AROW), aq = tk + L_Q + hh * 4096;
      u32x4 q0[2], q1[2];
#pragma unroll
      for (int a = 0; a < 2; ++a) { q0[a] = *(const LAS u32x4*)(aq + a * 512); q1[a] = *(const LAS u32x4*)(aq + 8192 + a * 512); }
      const u32x4 m0 = *(const LAS u32x4*)(L0 + L_M + lane * 16), m1 = *(const LAS u32x4*)(L0 + L_M + 1024 + lane * 16);
      const int G = (blk ? 4 : ((pp & 1) << 1) | (pp >> 1)) + par;
      const unsigned av = L0 + ((blk & (pp >> 1)) ? L_FR + 8 : (3 + (G >> 1)) * AROW + 8 * (G & 1)) + (64 * w + 4 * hh + q4) * 16;
      f32x16 o[2];
#pragma unroll 1
      for (int pass = 0; pass < 2; ++pass) {
        f32x16 S[2], sx;
        bf16x8 kf[4], vf[4];
        u32x4 pa[2], pb[2];
        auto krd = [&](int j, int i) { kf[j] = lds_b128(ak0 + i * 512); kf[j + 1] = lds_b128(ak1 + i * 512); };
        auto vrd = [&](int j, int i) {
          vf[j] = join(lds_tr(av + i * 512), lds_tr(av + i * 512 + 128));
          vf[j + 1] = join(lds_tr(av + i * 512 + 256), lds_tr(av + i * 512 + 384));
        };
        auto grp = [&](auto K_, auto G_) {
          constexpr int k = K_, gq = G_;
          float e[4];
          static_for<0, 4>([&](auto J_) {
            constexpr int r = 4 * gq + J_, kk = (r & 3) + 8 * (r >> 2);
            constexpr u64 msk = ((1ull << kk) - 1) | (((1ull << (kk + 4)) - 1) << 32);
            float v = S[k & 1][r];
            if constexpr (k >= 14) v = __builtin_amdgcn_inverse_ballot_w64(msk) ? v : sx[r];
            e[J_] = __builtin_amdgcn_exp2f(v);
          });
          u32x4& d = gq < 2 ? pa[k & 1] : pb[k & 1];
          d[2 * (gq & 1)] = pk2(e[0], e[1]); d[2 * (gq & 1) + 1] = pk2(e[2], e[3]);
          PIN(d);
          SBAR();
        };
        krd(0, 1); krd(2, 2);
        S[0] = mfma32(kf[0], asbf(q0[0]), (f32x16){});
        S[0] = mfma32(kf[1], asbf(q1[0]), S[0]);
        kf[0] = kf[2]; kf[1] = kf[3];
        SBAR();
        static_for<0, 17>([&](auto K_) {
          constexpr int k = K_, n = k + 1, pv = k - 1;
          if constexpr (k == 3) { if (pass == 0) vm0_bar(); }
          f32x16 sn, sy;
          if constexpr (n < 16) { sn = mfma32(kf[0], asbf(q0[n & 1]), (f32x16){}); PIN(sn); SBAR(); }
          if constexpr (pv >= 14) { o[pv & 1] = mfma32(vf[2], asbf(pa[pv & 1] & ~m0), o[pv & 1]); PIN(o[pv & 1]); SBAR(); }
          if constexpr (k < 16) grp(K_, IC<0>{});
          if constexpr (n < 16) { sn = mfma32(kf[1], asbf(q1[n & 1]), sn); PIN(sn); }
          if constexpr (n + 1 < 14) krd(0, ((n + 1) & 1) + 1 + ((n + 1) >> 1));
          else if constexpr (n + 1 < 16) krd(0, (n + 1) & 1);
          SBAR();
          if constexpr (pv >= 14) { o[pv & 1] = mfma32(vf[3], asbf(pb[pv & 1] & ~m1), o[pv & 1]); PIN(o[pv & 1]); SBAR(); }
          if constexpr (n == 14 || n == 15) { sy = mfma32(kf[2], asbf(q0[n & 1]), (f32x16){}); PIN(sy); SBAR(); }
          if constexpr (k < 16) grp(K_, IC<1>{});
          if constexpr (pv >= 0) {
            o[pv & 1] = mfma32(vf[0], asbf(pv >= 14 ? pa[pv & 1] & m0 : pa[pv & 1]), pv < 2 ? (f32x16){} : o[pv & 1]); PIN(o[pv & 1]); SBAR();
          }
          if constexpr (n == 14 || n == 15) { sy = mfma32(kf[3], asbf(q1[n & 1]), sy); PIN(sy); }
          if constexpr (n + 1 == 14 || n + 1 == 15) krd(2, ((n + 1) & 1) + 8);
          SBAR();
          if constexpr (k < 16) grp(K_, IC<2>{});
          if constexpr (pv >= 0) { o[pv & 1] = mfma32(vf[1], asbf(pv >= 14 ? pb[pv & 1] & m1 : pb[pv & 1]), o[pv & 1]); PIN(o[pv & 1]); }
          if constexpr (k < 14) vrd(0, (k & 1) + 1 + (k >> 1));
          else if constexpr (k < 16) { vrd(0, k & 1); vrd(2, (k & 1) + 8); }
          SBAR();
          if constexpr (k < 16) grp(K_, IC<3>{});
          if constexpr (n < 16) S[n & 1] = sn;
          if constexpr (n == 14 || n == 15) sx = sy;
        });
        if (pass || __all(o[0][12] < 1e18f && o[1][12] < 1e18f)) break;
#pragma unroll
        for (int a = 0; a < 2; ++a) {
          q1[a][0] = hh ? 0x0000f149u : q1[a][0];
          q1[a][1] = hh ? 0u : q1[a][1];
          float mx = -__builtin_inff();
#pragma unroll 1
          for (int i = 0; i < 9; ++i) {
            f32x16 t = mfma32(lds_b128(ak0 + (a + i) * 512), asbf(q0[a]), (f32x16){});
            t = mfma32(lds_b128(ak1 + (a + i) * 512), asbf(q1[a]), t);
#pragma unroll
            for (int r = 0; r < 16; ++r) {
              const unsigned dist = 256 + l31 - 32 * i - ((r & 3) + 8 * (r >> 2) + 4 * hh);
              mx = dist < 256u ? fmaxf(mx, t[r]) : mx;
            }
          }
          auto sw = __builtin_amdgcn_permlane32_swap(__float_as_uint(mx), __float_as_uint(mx), false, false);
          const u32x2 m3 = split3(-fmaxf(__uint_as_float(sw[0]), __uint_as_float(sw[1])));
          q1[a][0] = hh ? (m3[0] << 16) | 0xf149u : q1[a][0];
          q1[a][1] = hh ? (m3[0] >> 16) | (m3[1] << 16) : q1[a][1];
        }
      }
      __builtin_amdgcn_s_setprio(3);
      const unsigned so = (4 * qb + w) * A2_MT;
#pragma unroll
      for (int a = 0; a < 2; ++a) {
        const float il = __builtin_amdgcn_rcpf(o[a][12]);
        st16c<0>((u32x4){pk2(o[a][0] * il, o[a][1] * il), pk2(o[a][2] * il, o[a][3] * il), pk2(o[a][4] * il, o[a][5] * il), pk2(o[a][6] * il, o[a][7] * il)},
                 hh * 1024 + l31 * 16, srd_a2, so + (192 + 2 * head) * 1024 + a * 512);
        st8c<0>((u32x2){pk2(o[a][8] * il, o[a][9] * il), pk2(o[a][10] * il, o[a][11] * il)}, hh ? 0x7ff00000u : l31 * 8, srd_a2,
                so + 256 * 1024 + head * 512 + a * 256);
      }
    }
    STAMP(6);
    {
      const unsigned d_a = L0 + w * 4096, so_a = mt * A2_MT + w * 4096, l16 = lane * 16;
      auto adma = [&](auto R_) { constexpr int r = R_; dma16c_x4<a_slot(r), r * 16384>(d_a, l16, srd_a2, so_a); };
      bar();
      wait_vm<0>();
      if (lane == 0) put_flag(P.ws + WS_FLAGS + FL_3, (4 * qb + w) * 32 + head, tag);
      __builtin_amdgcn_s_setprio(1);
      STAMP(35);
      static_for<5, 9>(adma);
      const int id = 64 * w + lane;
      ld16<0>(xres[0], (id / 10) * 1280 + (id % 10) * 16, srd_x, mt * (64 * 1280) + jn * 160);
      ld16<0>(xres[1], w < 2 ? ((id + 512) / 10) * 1280 + ((id + 512) % 10) * 16 : 0x7ff00000u, srd_x, mt * (64 * 1280) + jn * 160);
      STAMP(36);
      wait_bar<2>();
      STAMP(37);
      static_for<9, 12>(adma);
      STAMP(38);
      poll_flags<32>(P.ws + WS_FLAGS + FL_3 + mt * 256, tag, lane);
      wait_vm<0>();
      PIN(xres[0]); PIN(xres[1]);
      STAMP(39);
      static_for<12, 14>(adma);
      bar();
      STAMP(40);
      static_for<14, 17>(adma);
      STAMP(41);
      wait_bar<16>(); wait_bar<12>(); wait_bar<8>(); wait_bar<4>(); wait_bar<0>();
      STAMP(7);
      bar();
    }
  } else {
    __builtin_amdgcn_s_setprio(2);
    const int s = w - 4;
    const int lane = fresh_lane();
    u32x2 ep;
    asm volatile("global_load_dwordx2 %0, %1, %2 sc1" : "=v"(ep) : "v"(0), "s"(P.ws + WS_FLAGS + FL_EPOCH) : "memory");
    const int rho = lane >> 3;
    const unsigned vo_x = (32 * (rho >> 1) + (rho & 1)) * 1280 + (lane & 7) * 16;
    const unsigned vo_q = ((rho & 1) + 4 * (rho >> 1)) * 3840 + (lane & 7) * 16;
    const unsigned vo_g = ((rho & 1) + 4 * (rho >> 1)) * 3072 + (lane & 7) * 16;
    const u32x4 srd_gu = make_srd((s >> 1) ? P.wu : P.wg, DM * FF * 2);
    const u32x4 srd_al = tyB ? srd_gu : make_srd(P.wqkv, DM * 3 * DM * 2);
    const u32x4 srd_be = make_srd(P.wqkv, ub < 30 ? DM * 3 * DM * 2 : 0);
    const unsigned st_al = tyB ? 3072 : 3840;
    unsigned vo_al[4];
#pragma unroll
    for (int m = 0; m < 4; ++m) vo_al[m] = (tyB ? vo_g : vo_q) + m * 16 * st_al;
    const unsigned c_x = (p * 128 + 8 * s) * 1280, c_al = (tyB ? ci : 3 * ci + (s >> 1)) * 128 + 2 * (s & 1) * st_al;
    const unsigned c_be = (ub < 30 ? ub : 0) * 128 + 2 * (s & 1) * 3840, c_ga = pg * 128 + 2 * (s & 1) * 3072;
    const unsigned d_x = L0 + s * 4160, d_2 = L0 + s * 4128, d_1 = L0 + (s & 1) * 8256 + (s >> 1) * 4096, d_s = L0 + s * 1024;
    const unsigned c_be2 = c_be + (s >> 1) * (64 * 3840);
    const bool ract = s < 3 && b * 3 + s < 680;
    const int ru = ract ? b * 3 + s : 0, rs = ru / 10, rbl = ru % 10;
    unsigned so_al = c_al;
    auto piece = [&](auto Q_) {
      constexpr int k = Q_ >> 2, m = Q_ & 3;
      constexpr unsigned d = upos(k);
      if constexpr (k < 20 && !(k & 1)) dma16<d + m * 1040, (k >> 1) * 128 + m * 2560>(d_x, vo_x, srd_x, c_x);
      else if constexpr (k < 20) {
        dma16<d + m * 1024, 0>(d_2, vo_al[m], srd_al, so_al);
        if constexpr (m == 3) { so_al += 64 * st_al; asm volatile("" : "+s"(so_al)); }
      } else if constexpr (k == 20) {
        const bool dn = rs < 48, tl = rs >= 64;
        const u32x4 srd_s = make_srd(dn ? P.wd : P.wo, !ract ? 0 : (dn ? FF * DM * 2 : DM * DM * 2));
        const unsigned vo = (tl ? 20 * (rho >> 2) + (rho & 3) : rho) * 1280 + (lane & 7) * 16;
        const int r0 = dn ? 32 * rs : (tl ? 160 * (rs - 64) + 16 : 40 * (rs - 48));
        const int d1 = tl ? 40 : 8, d2 = dn ? 16 : (tl ? 80 : 20);
        dma16<d + m * 1024, 0>(d_2, vo, srd_s, (r0 + (m & 1) * d1 + (m >> 1) * d2) * 1280 + rbl * 128);
      } else if constexpr (k < 26) dma16<d + m * 1024, ((k - 21) * 128 + m * 16) * 3840>(d_1, vo_q, srd_be, c_be2);
      else dma16<d + m * 1024, ((k - 26) * 64 + m * 16) * 3072>(d_2, vo_g, srd_gu, c_ga);
    };
    STAMP(60);
    static_for<0, 4 * PRO>(piece);

    u32x4 br[5 * NBR];
    const unsigned so_b = jn * R_J + s * 5120;
    const unsigned l16 = lane * 16;
    auto bload = [&](auto I_) { constexpr int i = I_; ld16cs<(i / 5) * 20480 + (i % 5) * 1024>(br[i % (5 * NBR)], l16, srd_r, so_b); };
    const int l31 = lane & 31, hh = lane >> 5;
    float cs[8];
    u32x4 stg;
    u64 tag = 0;

    static_for<0, 25>([&](auto I_) {
      constexpr int I = I_, k = I < 10 ? 2 * I + 1 : I + 11;
      if constexpr (FINE == 1) STAMP(8 + I);
      if constexpr (I == 17) wait_bar<0>(); else wait_bar<younger_unit(k, I)>();
      if constexpr (FINE == 1) STAMP(35 + I);
      if constexpr (I == 0) { PIN(ep); tag = uni64(ep) + 1; }
      if constexpr (I == 17) { if (s == 0 && lane == 0) put_flag(P.ws + WS_FLAGS + FL_1, b, tag); }
      constexpr bool beta = I >= 10 && I < 15, first = I == 0 || I == 10 || I == 15, last = I == 9 || I == 14 || I == 24;
      constexpr int q0 = 4 * fill_beg(I), nq = 4 * fill_end(I) - q0;
      auto pc = [&](auto J_) { if constexpr (J_ < nq) piece(IC<q0 + J_>{}); SBAR(); };
      u32x4 cf[4];
      {
        unsigned a = (beta ? d_1 : d_2) + upos(k) + l16; PIN(a);
#pragma unroll
        for (int i = 0; i < 4; ++i) cf[i] = *(const LAS u32x4*)(a + i * 1024);
      }
      if constexpr (first) {
#pragma unroll
        for (int j = 0; j < 8; ++j) cs[j] = 0.f;
      }
      SBAR();
      static_for<0, 8>([&](auto J_) {
        constexpr int j = J_, i = j >> 1, e = 2 * (j & 1);
        pc(J_);
        float t0, t1;
        asm volatile("v_lshlrev_b32 %4, 16, %6\n\tv_and_b32 %5, 0xffff0000, %6\n\tv_add_f32 %0, %0, %4\n\tv_add_f32 %1, %1, %5\n\t"
                     "v_lshlrev_b32 %4, 16, %7\n\tv_and_b32 %5, 0xffff0000, %7\n\tv_add_f32 %2, %2, %4\n\tv_add_f32 %3, %3, %5"
                     : "+v"(cs[2 * e]), "+v"(cs[2 * e + 1]), "+v"(cs[2 * e + 2]), "+v"(cs[2 * e + 3]), "=&v"(t0), "=&v"(t1) : "v"(cf[i][e]), "v"(cf[i][e + 1]));
        SBAR();
      });
      if constexpr (last) {
        const float a0 = swap_add32(cs[0], cs[4]), a1 = swap_add32(cs[1], cs[5]), a2 = swap_add32(cs[2], cs[6]), a3 = swap_add32(cs[3], cs[7]);
        const float b0 = dpp_add<0x128>(swap_add16(a0, a2)), b1 = dpp_add<0x128>(swap_add16(a1, a3));
        const int col = 8 * (lane & 7) + 4 * hh + 2 * ((lane >> 4) & 1) + ((lane >> 3) & 1), gq = (col >> 2) & 3;
        const int n = 4 * (((gq & 1) << 1) | (gq >> 1)) + (col & 3);
        const int e = beta ? ((col >> 5) + 2 * (s >> 1)) * 64 + (col & 16) + n : (col >> 4) * 64 + 16 * (s >> 1) + n;
        *(LAS u32x2*)(L0 + L_E + (e + 32 * (s & 1)) * 8) = split3((lane & 8) ? b1 : b0);
      }
      if constexpr (I == I_ST) ld16c<0>(stg, l31 * 16, srd_st, (p * 128 + 32 * s) * 16);
      if constexpr (I == 9) {
        wait_vm<ops_between(I_ST + 1, 10)>();
        PIN(stg);
        const unsigned tl = (unsigned)tag << 16, th = (unsigned)(tag >> 16);
#pragma unroll 1
        for (unsigned spin = 0; spin < SPIN_CAP; ++spin) {
          if (__all(((stg[0] ^ tl) | (stg[2] ^ tl)) < 65536u && stg[1] == th && stg[3] == th)) break;
          __builtin_amdgcn_s_sleep(1);
          ld16c<0>(stg, l31 * 16, srd_st, (p * 128 + 32 * s) * 16);
          wait_vm<0>();
          PIN(stg);
        }
        *(LAS u32x2*)(L0 + L_ST + (32 * s + l31) * 8) = (u32x2){(stg[0] & 0xffffu) | (stg[0] << 16), __float_as_uint(__builtin_amdgcn_rcpf(__uint_as_float(stg[2] << 16)))};
      }
      if constexpr (I == I_RP) {
        const unsigned ar = d_2 + upos(20) + (8 * (lane >> 4) + ((lane & 15) >> 2)) * 128 + 8 * (lane & 3);
        const unsigned vs = ract ? l16 : 0x7ff00000u;
#pragma unroll
        for (int t = 0; t < 4; ++t) {
          const int ct = 4 * rbl + t, cj = (ct * 13) >> 6;
          const bf16x8 f = join(lds_tr(ar + 32 * t), lds_tr(ar + 32 * t + 512));
          st16c<0>(__builtin_bit_cast(u32x4, f), vs, srd_r, ((cj * 68 + rs) * 5 + ct - 5 * cj) * 1024);
        }
      }
      if constexpr (I == 18) {
#pragma unroll
        for (int hb = 0; hb < 2; ++hb)
          *(LAS u32x4*)(L0 + L_FR + (256 * hb + 64 * s) * 16 + l16) = (u32x4){(qb == 0 && hb == 0) ? 0x3f803f80u : 0x3f800000u, 0x3f803f80u, 0x3f803f80u, 0x3f803f80u};
        const int k0 = 2 * (s & 1) + 8 * (s >> 1) + 4 * hh;
        *(LAS unsigned*)(L0 + L_M + l16 + 4 * s) = (k0 > l31 ? 0xffffu : 0u) | (k0 + 1 > l31 ? 0xffff0000u : 0u);
        *(LAS unsigned*)(L0 + L_M + 1024 + l16 + 4 * s) = (k0 + 16 > l31 ? 0xffffu : 0u) | (k0 + 17 > l31 ? 0xffff0000u : 0u);
      }
      if constexpr (I == I_KQ) {
        poll_flags<256>(P.ws + WS_FLAGS + FL_1, tag, lane);
        if (b == 0 && s == 0 && lane == 0) put_flag(P.ws + WS_FLAGS + FL_EPOCH, 0, tag);
      }
      if constexpr (I == I_KQ || I == I_V) {
        const unsigned s1o = (4 * qb + s) * QKV_TB + o0 * 1024, s0o = qb ? s1o - 4 * QKV_TB : s1o;
        constexpr int row = I == I_V ? 3 : 0, sec = I == I_V ? 160 : 80;
        static_for<0, 3>([&](auto J_) {
          constexpr int i = J_;
          if constexpr (I == I_KQ) dma16c<L_Q + i * 4096, i * 1024>(d_s, l16, srd_qkv, s1o);
          dma16c<(row + i) * AROW + 4096, (sec + i) * 1024>(d_s, l16, srd_qkv, s1o);
        });
        static_for<0, 3>([&](auto J_) { constexpr int i = J_; dma16c<(row + i) * AROW, (sec + i) * 1024>(d_s, l16, srd_qkv, s0o); });
      }
      static_for<bl_end(I - 1), bl_end(I)>(bload);
      SBAR();
    });
    STAMP(1);
    lgkm_bar();
    {
      wait_vm<3 + ops_between(I_KQ + 1, 25)>();
      const unsigned aq = d_s + L_Q + l31 * 16, ak = d_s + 4096 + l31 * 16;
#pragma unroll
      for (int a = 0; a < 2; ++a) {
        u32x4 q0 = *(const LAS u32x4*)(aq + hh * 4096 + a * 512), q1 = *(const LAS u32x4*)(aq + 8192 + a * 512);
        const bool z0 = par && !hh;
        q0[0] = z0 ? 0u : q0[0]; q0[1] = z0 ? 0u : q0[1];
        q1[0] = hh ? 0u : q1[0]; q1[1] = hh ? 0u : q1[1];
        q1[2] = z0 ? q1[2] : 0u; q1[3] = z0 ? q1[3] : 0u;
        f32x16 t = mfma32(lds_b128(ak + hh * AROW + a * 512), asbf(q0), (f32x16){});
        t = mfma32(lds_b128(ak + 2 * AROW + a * 512), asbf(q1), t);
        float mx = -__builtin_inff();
        static_for<0, 8>([&](auto R_) {
          constexpr int r = 2 * R_, kk = (r & 3) + 8 * (r >> 2);
          constexpr u64 ma = ((1ull << kk) - 1) | (((1ull << (kk + 4)) - 1) << 32), mb = ((1ull << (kk + 1)) - 1) | (((1ull << (kk + 5)) - 1) << 32);
          mx = max3(mx, __builtin_amdgcn_inverse_ballot_w64(ma) ? -__builtin_inff() : t[r], __builtin_amdgcn_inverse_ballot_w64(mb) ? -__builtin_inff() : t[r + 1]);
        });
        auto sw = __builtin_amdgcn_permlane32_swap(__float_as_uint(mx), __float_as_uint(mx), false, false);
        const u32x2 m3 = split3(-max3(mx, __uint_as_float(sw[0]), __uint_as_float(sw[1])));
        q1[0] = hh ? (m3[0] << 16) | 0xf149u : q1[0];
        q1[1] = hh ? (m3[0] >> 16) | (m3[1] << 16) : q1[1];
        *(LAS u32x4*)(aq + hh * 4096 + a * 512) = q0;
        *(LAS u32x4*)(aq + 8192 + hh * 4096 + a * 512) = q1;
      }
    }
    wait_vm<ops_between(I_V + 1, 25)>();
    STAMP(2);
    lgkm_bar();
    STAMP(3);
    const unsigned d_a = L0 + s * 4096, so_a = mt * A2_MT + s * 4096;
    auto adma = [&](auto R_) { constexpr int r = R_; dma16c_x4<a_slot(r), r * 16384>(d_a, l16, srd_a2, so_a); };
    static_for<bl_end(24), bl_end(24) + 5>(bload);
    bar();
    if (s == 0 && lane == 0) put_flag(P.ws + WS_FLAGS + FL_2, p * 16 + c, tag);
    static_for<0, 4>(adma);
    static_for<bl_end(24) + 5, 5 * NBR>(bload);
    STAMP(4);
    poll_flags<16>(P.ws + WS_FLAGS + FL_2 + (mt >> 1) * 128, tag, lane);
    wait_vm<0>();
    adma(IC<4>{});
    STAMP(5);
    {
      unsigned aa = L0 + (4 * s + (lane >> 4)) * 1024 + (lane & 15) * 16;
      PIN(aa);
      bf16x8 af[2][4];
      f32x4 acc[4][5];
      unsigned cur;
      static_for<0, 18>([&](auto R_) {
        constexpr int r = R_, q = r - 1;
        if constexpr (r == 0 || r == 5 || r == 9 || (r >= 12 && r < 17)) {
          if constexpr (FINE == 2) STAMP(8 + r);
          lgkm_bar();
          if constexpr (FINE == 2) STAMP(35 + r);
        }
        if constexpr (r == 4) wait_vm<15>();
        if constexpr (r < 16) { cur = aa + a_slot(r); PIN(cur); }
        if constexpr (r == 16) { const int ln = fresh_lane(); cur = L0 + a_slot(r) + (8 * s + 2 * (ln >> 4)) * 512 + (ln & 15) * 8; PIN(cur); }
        if constexpr (q >= NBR) wait_vm<5 * (imin(q + NBR, 17) - q - 1)>();
        if constexpr (r == 13) {
          const int id = 64 * w + fresh_lane();
          ld16<0>(xres[0], (id / 10) * 1280 + (id % 10) * 16, srd_x, mt * (64 * 1280) + jn * 160);
        }
        if constexpr (r == 17) { PIN(xres[0]); xres[1] = (u32x4){0u, 0u, 0u, 0u}; }
        SBAR();
        static_for<0, 20>([&](auto M_) {
          constexpr int m = M_, t = m >> 2, i = m & 3;
          if constexpr (q >= 0) {
            u32x4& wv = br[(5 * q + t) % (5 * NBR)];
            if constexpr (i == 0) PIN(wv);
            acc[i][t] = mfma16(asbf(wv), af[q & 1][i], q == 0 ? (f32x4){} : acc[i][t]);
            SBAR();
            if constexpr (i == 3 && q + NBR < 17) { bload(IC<5 * (q + NBR) + t>{}); SBAR(); }
          }
          if constexpr (r < 17 && m >= 1 && m < 10 && (m & 1) && m / 2 < 4) {
            constexpr int j = m / 2;
            if constexpr (r < 16) af[r & 1][j] = lds_b128(cur + j * 256);
            else {
              const u32x2 lo = *(const LAS u32x2*)(cur + j * 128), hi = *(const LAS u32x2*)(cur + j * 128 + 512);
              af[r & 1][j] = asbf((u32x4){lo[0], lo[1], hi[0], hi[1]});
            }
            SBAR();
          }
        });
      });
      STAMP(7);
      bar();
      const int ln = fresh_lane();
      unsigned ap = L0 + s * PCLS + (ln & 15) * PROW + 16 * (ln >> 4);
      PIN(ap);
#pragma unroll
      for (int t = 0; t < 5; ++t)
#pragma unroll
        for (int i = 0; i < 4; ++i) *(LAS f32x4*)(ap + 16 * i * PROW + 64 * t) = acc[i][t];
    }
  }
  wait_bar<0>();
  PIN(xres[0]); PIN(xres[1]);
  {
    const int lane = fresh_lane();
#pragma unroll
    for (int it = 0; it < 2; ++it) {
      if (it == 0 || w < 2) {
        const int id = 64 * w + lane + 512 * it, row = id / 10, cc = id % 10;
        f32x4 lo = {}, hi = {};
#pragma unroll
        for (int k = 0; k < 4; ++k) {
          lo += *(const LAS f32x4*)(L0 + k * PCLS + row * PROW + cc * 32);
          hi += *(const LAS f32x4*)(L0 + k * PCLS + row * PROW + cc * 32 + 16);
        }
        const u32x4 xv = xres[it];
        u32x4 ov;
        ov[0] = pk2(lo[0] + __uint_as_float(xv[0] << 16), lo[1] + __uint_as_float(xv[0] & 0xffff0000u));
        ov[1] = pk2(lo[2] + __uint_as_float(xv[1] << 16), lo[3] + __uint_as_float(xv[1] & 0xffff0000u));
        ov[2] = pk2(hi[0] + __uint_as_float(xv[2] << 16), hi[1] + __uint_as_float(xv[2] & 0xffff0000u));
        ov[3] = pk2(hi[2] + __uint_as_float(xv[3] << 16), hi[3] + __uint_as_float(xv[3] & 0xffff0000u));
        *(GAS u32x4*)((char*)P.out + (size_t)(64 * mt + row) * 1280 + jn * 160 + cc * 16) = ov;
      }
    }
  }
  asm volatile(".Lcode_end:");
}

extern "C" void kernel_launch(void* const* d_in, const int* in_sizes, int n_in, void* d_out, int out_size, void* d_ws, size_t ws_size,
                              hipStream_t stream) {
  Params P;
  P.x = d_in[0]; P.wqkv = d_in[1]; P.wo = d_in[2]; P.wg = d_in[4]; P.wu = d_in[5]; P.wd = d_in[6];
  P.out = d_out; P.ws = (char*)d_ws;
  fused<<<256, 512, 0, stream>>>(P);
}
```

```cpp
#include <hip/hip_runtime.h>
#include <stdint.h>
#include <type_traits>
#include <utility>

#define STAMPS 0
#define FINE 0
#define ABL 0
#define PRO 4
#define NB1 15
#define ZF1 28
#define KF2 3
#define CSV 2
#define POLL_SLEEP 3
#define RT 5
#define NLAUNCH 1

typedef __bf16 bf16x8 __attribute__((ext_vector_type(8)));
typedef __bf16 bf16x2 __attribute__((ext_vector_type(2)));
typedef short s16x4 __attribute__((ext_vector_type(4)));
typedef float f32x16 __attribute__((ext_vector_type(16)));
typedef float f32x4 __attribute__((ext_vector_type(4)));
typedef float f32x2 __attribute__((ext_vector_type(2)));
typedef unsigned u32x4 __attribute__((ext_vector_type(4)));
typedef unsigned u32x2 __attribute__((ext_vector_type(2)));
typedef unsigned u32x16 __attribute__((ext_vector_type(16)));
typedef unsigned long long u64;
#define LAS __attribute__((address_space(3)))
#define GAS __attribute__((address_space(1)))
#define DEV __device__ __forceinline__
#define INL __attribute__((always_inline))
template <int V> using IC = std::integral_constant<int, V>;

constexpr int SEQ = 2048, DM = 640, FF = 1536;
constexpr unsigned OOB = 0x7ff00000u;
constexpr unsigned QKV_TB = 240 * 1024;
constexpr unsigned A2_MT = 272 * 1024;
constexpr unsigned R_J = 68 * 5 * 1024;
constexpr unsigned FL_EPOCH = 0, FL_0 = 2048, FL_1 = 8192, FL_2 = 16384, FL_3 = 24576, FL_REP = 32768;
constexpr unsigned WS_STAT = 8 * FL_REP, WS_CS = WS_STAT + 32768, WS_QKV = 524288, WS_A2 = WS_QKV + 32 * QKV_TB, WS_R = WS_A2 + 32 * A2_MT, WS_STAMP = WS_R + 8 * R_J;

constexpr unsigned UNIT = 16640;
constexpr unsigned upos(int k) { return ((k + 7) % 9) * UNIT; }
constexpr unsigned AROW = 8256;
constexpr unsigned L_FR = 2 * AROW, L_K2 = 8 * UNIT, L_Q = L_K2 + 8192;
constexpr unsigned krow(int i) { return i < 2 ? i * AROW : L_K2; }
constexpr unsigned L_E = 160768, L_ST = L_E + 2048;
static_assert(AROW + 8192 <= UNIT && L_Q + 16384 <= L_E && L_E >= 9 * UNIT, "attention image");
constexpr unsigned L_A0 = 6 * AROW;
static_assert(L_A0 + 5 * 16384 <= L_K2, "phase-2 image");
constexpr int a_idx(int r) { return r < 13 ? r - 4 : r - 13; }
constexpr unsigned a_slot(int r) { return a_idx(r) < 6 ? L_A0 + a_idx(r) * 16384 : (a_idx(r) - 6) * 16384; }
constexpr unsigned PROW = 336, PCLS = 64 * PROW;
constexpr unsigned L_P3 = L_A0 + 4 * 16384;
static_assert(3 * PCLS <= a_slot(14) && a_slot(16) + 16384 <= L_P3, "partial sums clear of the last rounds");

template <int I, class F, int... J> DEV void static_for_impl(F& f, std::integer_sequence<int, J...>) { (f(IC<I + J>{}), ...); }
template <int I, int N, class F> DEV void static_for(F&& f) { if constexpr (I < N) static_for_impl<I>(f, std::make_integer_sequence<int, N - I>{}); }

template <unsigned LOFF, unsigned SOFF> DEV void dma16(unsigned lb, unsigned voff, u32x4 srd, unsigned sb) {
  unsigned t;
  asm volatile("s_add_u32 m0, %1, %5\n\ts_add_u32 %0, %4, %6\n\tbuffer_load_dwordx4 %2, %3, %0 offen lds"
               : "=&s"(t) : "s"(lb), "v"(voff), "s"(srd), "s"(sb), "i"(LOFF), "i"(SOFF) : "memory", "scc");
}
template <unsigned LOFF, unsigned SOFF> DEV void dma16c(unsigned lb, unsigned voff, u32x4 srd, unsigned sb) {
  unsigned t;
  asm volatile("s_add_u32 m0, %1, %5\n\ts_add_u32 %0, %4, %6\n\tbuffer_load_dwordx4 %2, %3, %0 offen sc1 lds"
               : "=&s"(t) : "s"(lb), "v"(voff), "s"(srd), "s"(sb), "i"(LOFF), "i"(SOFF) : "memory", "scc");
}
template <unsigned LOFF> DEV void dma16c_x4(unsigned lb, unsigned voff, u32x4 srd, unsigned sb) {
  asm volatile("s_add_u32 m0, %0, %4\n\ts_nop 0\n\tbuffer_load_dwordx4 %1, %2, %3 offen sc1 lds\n\t"
               "buffer_load_dwordx4 %1, %2, %3 offen offset:1024 sc1 lds\n\tbuffer_load_dwordx4 %1, %2, %3 offen offset:2048 sc1 lds\n\t"
               "buffer_load_dwordx4 %1, %2, %3 offen offset:3072 sc1 lds"
               :: "s"(lb), "v"(voff), "s"(srd), "s"(sb), "i"(LOFF) : "memory", "scc");
}
template <int OFF> DEV void ld16c(u32x4& v, unsigned voff, u32x4 srd, unsigned soff) {
  asm volatile("buffer_load_dwordx4 %0, %1, %2, %3 offen offset:%4 sc1" : "=v"(v) : "v"(voff), "s"(srd), "s"(soff), "i"(OFF) : "memory");
}
template <int OFF> DEV void ld8c(u32x2& v, unsigned voff, u32x4 srd, unsigned soff) {
  asm volatile("buffer_load_dwordx2 %0, %1, %2, %3 offen offset:%4 sc1" : "=v"(v) : "v"(voff), "s"(srd), "s"(soff), "i"(OFF) : "memory");
}
template <int OFF> DEV void ld16(u32x4& v, unsigned voff, u32x4 srd, unsigned soff) {
  asm volatile("buffer_load_dwordx4 %0, %1, %2, %3 offen offset:%4" : "=v"(v) : "v"(voff), "s"(srd), "s"(soff), "i"(OFF) : "memory");
}
template <int OFF> DEV void st16c(u32x4 v, unsigned voff, u32x4 srd, unsigned soff) {
  asm volatile("buffer_store_dwordx4 %0, %1, %2, %3 offen offset:%4 sc1\n\ts_nop 1" ::"v"(v), "v"(voff), "s"(srd), "s"(soff), "i"(OFF) : "memory");
}
template <int OFF> DEV void st16(u32x4 v, unsigned voff, u32x4 srd, unsigned soff) {
  asm volatile("buffer_store_dwordx4 %0, %1, %2, %3 offen offset:%4\n\ts_nop 1" ::"v"(v), "v"(voff), "s"(srd), "s"(soff), "i"(OFF) : "memory");
}
template <unsigned SOFF> DEV void st16p(u32x4 v, unsigned voff, u32x4 srd, unsigned sb) {
  unsigned t;
  asm volatile("s_setprio 3\n\ts_add_u32 %0, %4, %5\n\tbuffer_store_dwordx4 %1, %2, %3, %0 offen sc1\n\ts_setprio 0\n\ts_nop 0"
               : "=&s"(t) : "v"(v), "v"(voff), "s"(srd), "s"(sb), "i"(SOFF) : "memory", "scc");
}
template <int OFF> DEV void st8c(u32x2 v, unsigned voff, u32x4 srd, unsigned soff) {
  asm volatile("buffer_store_dwordx2 %0, %1, %2, %3 offen offset:%4 sc1" ::"v"(v), "v"(voff), "s"(srd), "s"(soff), "i"(OFF) : "memory");
}
template <int OFF> DEV void st4c(unsigned v, unsigned voff, u32x4 srd, unsigned soff) {
  asm volatile("buffer_store_dword %0, %1, %2, %3 offen offset:%4 sc1" ::"v"(v), "v"(voff), "s"(srd), "s"(soff), "i"(OFF) : "memory");
}
template <int N> DEV void wait_vm() { asm volatile("s_waitcnt vmcnt(%0)" ::"i"(N > 63 ? 63 : N) : "memory"); }
template <int N> DEV void wait_all() { asm volatile("s_waitcnt vmcnt(%0) lgkmcnt(0)" ::"i"(N > 63 ? 63 : N) : "memory"); }
template <int N> DEV void wait_bar() { asm volatile("s_waitcnt vmcnt(%0) lgkmcnt(0)\n\ts_barrier" ::"i"(N > 63 ? 63 : N) : "memory"); }
DEV void bar() { asm volatile("s_barrier" ::: "memory"); }
#define PIN(x) asm volatile("" : "+v"(x))
#define PINS(x) asm volatile("" : "+s"(x))
#define SBAR() __builtin_amdgcn_sched_barrier(0)
DEV int fresh_lane() { int l; asm volatile("v_mbcnt_lo_u32_b32 %0, -1, 0\n\tv_mbcnt_hi_u32_b32 %0, -1, %0" : "=v"(l)); return l; }

DEV bf16x8 lds_b128(unsigned a) { return *(const LAS bf16x8*)a; }
DEV s16x4 lds_tr(unsigned a) { return __builtin_amdgcn_ds_read_tr16_b64_v4i16((LAS s16x4*)a); }
DEV bf16x8 join(s16x4 lo, s16x4 hi) { return __builtin_bit_cast(bf16x8, __builtin_shufflevector(lo, hi, 0, 1, 2, 3, 4, 5, 6, 7)); }
DEV bf16x8 asbf(u32x4 v) { return __builtin_bit_cast(bf16x8, v); }
DEV f32x16 mfma32(bf16x8 a, bf16x8 b, f32x16 c) { return __builtin_amdgcn_mfma_f32_32x32x16_bf16(a, b, c, 0, 0, 0); }
DEV f32x4 mfma16(bf16x8 a, bf16x8 b, f32x4 c) { return __builtin_amdgcn_mfma_f32_16x16x32_bf16(a, b, c, 0, 0, 0); }
DEV unsigned pk2(float lo, float hi) { return __builtin_bit_cast(unsigned, (bf16x2){(__bf16)lo, (__bf16)hi}); }
DEV float rbf(float x) { return (float)(__bf16)x; }
DEV float max3(float a, float b, float c) { float r; asm("v_max3_f32 %0, %1, %2, %3" : "=v"(r) : "v"(a), "v"(b), "v"(c)); return r; }
template <int J> DEV u32x4 piece4(f32x16 a) { return __builtin_bit_cast(u32x4, __builtin_shufflevector(a, a, 4 * J, 4 * J + 1, 4 * J + 2, 4 * J + 3)); }
DEV float relu(float x, int lo) { const int i = __float_as_int(x); return __int_as_float(i > lo ? i : lo); }
DEV u32x2 split3(float c) { const float a = rbf(c), b = rbf(c - a), d = c - a - b; return (u32x2){pk2(a, b), pk2(d, 0.f)}; }
template <int CTRL> DEV float dpp_add(float x) { return x + __int_as_float(__builtin_amdgcn_update_dpp(0, __float_as_int(x), CTRL, 0xf, 0xf, true)); }
DEV float swap_add32(float a, float b) { auto r = __builtin_amdgcn_permlane32_swap(__float_as_uint(a), __float_as_uint(b), false, false); return __uint_as_float(r[0]) + __uint_as_float(r[1]); }
DEV float swap_add16(float a, float b) { auto r = __builtin_amdgcn_permlane16_swap(__float_as_uint(a), __float_as_uint(b), false, false); return __uint_as_float(r[0]) + __uint_as_float(r[1]); }
DEV float swap_max32(float a) { auto r = __builtin_amdgcn_permlane32_swap(__float_as_uint(a), __float_as_uint(a), false, false); return max3(a, __uint_as_float(r[0]), __uint_as_float(r[1])); }
DEV u64 uni64(u32x2 v) { return ((u64)(unsigned)__builtin_amdgcn_readfirstlane(v[1]) << 32) | (unsigned)__builtin_amdgcn_readfirstlane(v[0]); }
constexpr u64 edge_mask(int r) { const int kk = (r & 3) + 8 * (r >> 2); return ((1ull << kk) - 1) | (((1ull << (kk + 4)) - 1) << 32); }

struct Buf { const void* p; unsigned bytes, mode; };
struct Params { Buf x, wqkv, wg, wu, ws, wd, wo, out; };

#define STAMP(i) do {} while (0)

constexpr unsigned SPIN_CAP = 200000;
DEV bool tagged(u32x4 v, u64 tag) { return (((v[0] ^ (unsigned)tag) | (v[2] ^ (unsigned)tag)) | ((v[1] ^ (unsigned)(tag >> 32)) | (v[3] ^ (unsigned)(tag >> 32)))) == 0u; }
DEV void poll(u32x4 ws, unsigned va, unsigned sa, u64 tag) {
#pragma unroll 1
  for (unsigned spin = 0; spin < SPIN_CAP; ++spin) {
    u32x4 a;
    ld16c<0>(a, va, ws, sa);
    wait_vm<0>(); PIN(a);
    if (__all(tagged(a, tag))) break;
    __builtin_amdgcn_s_sleep(POLL_SLEEP);
  }
}
DEV void put_flag(u32x4 ws, unsigned off, u64 tag, int copies = 8) {
  const int lane = fresh_lane();
  st8c<0>((u32x2){(unsigned)tag, (unsigned)(tag >> 32)}, lane < copies ? lane * FL_REP : OOB, ws, off);
}

constexpr int U_RP = 27, U_CS = 28;
constexpr int imin(int a, int b) { return a < b ? a : b; }
constexpr int wunit(int I) { return I < 10 ? 2 * I + 1 : (I < 17 ? I + 10 : I + 12); }
constexpr bool has_bar(int I) { return I <= 7 || I == 9 || I == 10 || I == 12 || I == 15 || I == 17 || I == 19 || I == 21; }
constexpr int need_unit(int I) { int J = I + 1; while (J < 25 && !has_bar(J)) ++J; return wunit(J - 1); }
constexpr int fill_end(int I) {
  if (I >= 8) return I < 9 ? 22 : (I < 10 ? 24 : (I < 12 ? 27 : (I < 15 ? 30 : (I < 17 ? 33 : (I < 19 ? 35 : 37)))));
  int e = PRO; for (int i = 0; i <= I; ++i) e = imin(e + (i < 3 ? 3 : 2), 2 * i + 8 < PRO ? PRO : 2 * i + 8); return e;
}
constexpr int fill_beg(int I) { return fill_end(I - 1); }
constexpr int I_ST = 7;
constexpr int extra_ops(int I) { return I == I_ST ? 1 : (I == 12 ? 5 : 0); }
constexpr int ops_in(int I) { return 4 * (fill_end(I) - fill_beg(I)) + extra_ops(I); }
constexpr int ops_between(int I0, int I1) { int n = 0; for (int i = I0; i < I1; ++i) n += ops_in(i); return n; }
static_assert(fill_end(7) == 22, "alpha fills");
constexpr int younger_unit(int k, int J) {
  if (k < PRO) return 4 * (PRO - 1 - k) + ops_between(0, J);
  int I = 0; while (fill_end(I) <= k) ++I;
  return ops_in(I) - 4 * (k + 1 - fill_beg(I)) + ops_between(I + 1, J);
}

#define ROLE_SETUP()                                                                                                                              \
  u32x16 ka, kb;                                                                                                                                  \
  asm volatile("s_load_dwordx16 %0, %2, 0x0\n\ts_load_dwordx16 %1, %2, 0x40" : "=&s"(ka), "=&s"(kb) : "s"(__builtin_amdgcn_kernarg_segment_ptr())); \
  int b = blockIdx.x; PINS(b);                                                                                                                    \
  const int xcd = b & 7, rr = b >> 3;                                                                                                             \
  const int p = (xcd >> 2) * 8 + (rr & 7), c = (xcd & 3) * 4 + (rr >> 3);                                                                         \
  const int tyB = c & 1, ci = c >> 1;                                                                                                             \
  const int ub = 3 * ci + 2 + tyB * (22 - 2 * ci), pg = 16 + ci - 8 * tyB;                                                                        \
  const int head = 4 * xcd + (rr & 3), qb = rr >> 2, par = head & 1, o0 = (5 * head) >> 1;                                                        \
  const int mt = (xcd >> 1) * 8 + (rr & 7), jn = (xcd & 1) * 4 + (rr >> 3);                                                                       \
  auto rnd = [&](int r) INL {                                                                                                                     \
    int v = r < 4 ? (r + jn + mt) & 3 : (r < 12 ? 4 + ((r + 2 * jn + mt) & 7) : (r < 16 ? 12 + ((r + jn + mt) & 3) : 16));                        \
    PINS(v);                                                                                                                                      \
    return v;                                                                                                                                     \
  }

__global__ __launch_bounds__(512, 2) void fused(Params P) {
#if defined(__HIP_DEVICE_COMPILE__)
  {
    unsigned c0, c1, len;
    asm volatile("s_getpc_b64 s[98:99]\n.Lpc_here:\n\ts_sub_u32 %0, s98, .Lpc_here-_Z5fused6Params\n\ts_subb_u32 %1, s99, 0\n\ts_mov_b32 %2, .Lcode_end-_Z5fused6Params"
                 : "=s"(c0), "=s"(c1), "=s"(len) :: "s98", "s99", "scc");
    const u32x4 srd_c = {c0, c1 & 0xffffu, len, 0x00020000u};
    asm volatile("buffer_load_dword v255, %0, %1, 0 offen" :: "v"(threadIdx.x * 2048 + (blockIdx.x & 120) * 16), "s"(srd_c) : "memory", "v255");
    SBAR();
  }
#define ARGS_READY() asm volatile("s_waitcnt lgkmcnt(0)" : "+s"(ka), "+s"(kb))
#define SRD(v, i) __builtin_shufflevector(v, v, 4 * (i), 4 * (i) + 1, 4 * (i) + 2, 4 * (i) + 3)
  __shared__ __attribute__((aligned(1024))) char smem[163840];
  const unsigned L0 = (unsigned)(uintptr_t)(LAS char*)smem;
  const int w = __builtin_amdgcn_readfirstlane(threadIdx.x >> 6);
  u32x4 xres[2], srd_o;
  unsigned so_out;

  if (w < 4) {
    ROLE_SETUP();
    ARGS_READY();
    const u32x4 srd_ws = SRD(kb, 0), srd_x = SRD(ka, 0);
    srd_o = SRD(kb, 3);
    so_out = mt * (64 * 1280) + jn * 160;
    const int tb = 2 * p + (w >> 1);
    u64 tag;
    f32x16 acc[4];
    unsigned vo_st;
    unsigned ak0, ak1, av, aq;
    f32x16 mlo, mhi;
    {
      constexpr int D = 5;
      bf16x8 xf[40], wf[D];
      unsigned ax, aw;
      {
        const int lane = fresh_lane(), l31 = lane & 31, hh = lane >> 5;
        ax = L0 + (l31 >> 1) * 1040 + (2 * w + (l31 & 1)) * 128 + hh * 16;
      }
      auto set_aw = [&](bool beta) INL {
        const int lane = fresh_lane(), hh = lane >> 5, blk = (lane >> 4) & 1, q = (lane & 15) >> 2, pp = lane & 3;
        const int pi = ((pp & 1) << 1) | (pp >> 1);
        aw = L0 + (q & 1) * 128 + hh * 512 + 8 * pi + (beta ? (q >> 1) * 8256 + 32 * blk : (2 * blk + (q >> 1)) * 4128);
        PIN(aw);
      };
      set_aw(false);
      unsigned cur, cx;
      u32x2 mf;
      float rstd;
      u32x2 ep;
      u32x4 xv[3];
      const int srow = p * 128 + 8 * c + 2 * w;
      auto stat_load = [&]() INL {
        const int lane = fresh_lane(), l31 = lane & 31;
        const unsigned vo = (lane >> 5) * 1280 + l31 * 16;
        __builtin_amdgcn_s_setprio(3);
        ld8c<FL_EPOCH>(ep, 0u, srd_ws, 0u);
        ld16<0>(xv[0], vo, srd_x, srow * 1280);
        ld16<512>(xv[1], vo, srd_x, srow * 1280);
        ld16<1024>(xv[2], l31 < 16 ? vo : OOB, srd_x, srow * 1280);
        __builtin_amdgcn_s_setprio(0);
      };
      auto stat_put = [&]() INL {
        wait_vm<0>();
        PIN(ep); PIN(xv[0]); PIN(xv[1]); PIN(xv[2]);
        tag = uni64(ep) + 1;
        float s1 = 0.f, s2 = 0.f;
        const bf16x2 one2 = __builtin_bit_cast(bf16x2, 0x3f803f80u);
#pragma unroll
        for (int i = 0; i < 12; ++i) {
          const unsigned raw = xv[i >> 2][i & 3];
          const bf16x2 v = __builtin_bit_cast(bf16x2, raw);
          s1 = __builtin_amdgcn_fdot2_f32_bf16(v, one2, s1, false);
          s2 = __builtin_amdgcn_fdot2_f32_bf16(v, v, s2, false);
        }
        s1 = dpp_add<0xb1>(s1); s2 = dpp_add<0xb1>(s2);
        s1 = dpp_add<0x4e>(s1); s2 = dpp_add<0x4e>(s2);
        s1 = dpp_add<0x141>(s1); s2 = dpp_add<0x141>(s2);
        s1 = dpp_add<0x140>(s1); s2 = dpp_add<0x140>(s2);
        s1 = swap_add16(s1, s1); s2 = swap_add16(s2, s2);
        const float mean = s1 * (1.f / DM), var = fmaxf(s2 * (1.f / DM) - mean * mean, 0.f);
        const unsigned pay = pk2(-mean, __builtin_amdgcn_sqrtf(rbf(rbf(var) + 1e-5f)));
        const int lane = fresh_lane(), l31 = lane & 31;
        const u32x2 gr = {((unsigned)tag << 16) | ((lane & 1) ? pay >> 16 : pay & 0xffffu), (unsigned)(tag >> 16)};
        __builtin_amdgcn_s_setprio(3);
        st8c<0>(gr, l31 < 2 ? (lane >> 5) * 16 + l31 * 8 : OOB, srd_ws, WS_STAT + srow * 16);
        __builtin_amdgcn_s_setprio(0);
      };
      constexpr auto z2f = [](int z) constexpr { return z < 160 ? z : (z < 164 ? -1 : (z < 244 ? z - 4 : (z < 248 ? -1 : (z < 408 ? z - 8 : -1)))); };
      constexpr auto zct = [](int z) constexpr { return z < 164 ? z - 160 : (z < 248 ? z - 244 : z - 408); };
      auto rd = [&](auto Z_) {
        constexpr int z = Z_, f = z2f(z);
        if constexpr (f < 0) {
          constexpr int t = zct(z);
          if constexpr (t == 0) {
            const int lane = fresh_lane();
            cur = L0 + L_E + lane * 8; PIN(cur);
            cx = L0 + L_ST + w * 256 + (lane & 31) * 8; PIN(cx);
            bar();
            mf = *(const LAS u32x2*)cx;
          }
          if constexpr (t == 2 && z < 400) set_aw(z < 200);
          const u32x2 e = *(const LAS u32x2*)(cur + (z > 200 && z < 300 ? t : t ^ 2) * 512);
          wf[z % D] = asbf((u32x4){e[0], e[1], e[0], e[1]});
        } else {
          constexpr int wu = f >> 4, i = f & 15, k = wunit(wu);
          if constexpr (i == 0) {
            cur = aw + upos(k); PIN(cur);
            if constexpr (wu < 10) { cx = ax + upos(k - 1); PIN(cx); }
            if constexpr (wu == 2) { SBAR(); stat_put(); SBAR(); }
            if constexpr (has_bar(wu) && wu != 10 && wu != 15) bar();
          }
          if constexpr (wu < 10 && (i == 0 || (i & 3) == 2) && i < 12) xf[4 * wu + ((i + 2) >> 2)] = lds_b128(cx + 32 * ((i + 2) >> 2));
          constexpr bool beta = wu >= 10 && wu < 15;
          constexpr int off = beta ? (i >> 1) * 1024 + (i & 1) * 64 : (i >> 2) * 1024 + (i & 3) * 32;
          wf[z % D] = join(lds_tr(cur + off), lds_tr(cur + off + 256));
        }
      };
      auto epi = [&](auto PASS_) {
        constexpr int pass = PASS_;
        constexpr float QS = 0.22360679774997896f * 1.4426950408889634f;
        const int lane = fresh_lane();
        vo_st = (lane >> 5) * 1024 + (32 * (w & 1) + (lane & 31)) * 16;
        if constexpr (pass == 0) {
          const float s1 = rstd * (tyB ? rstd : (3 * ci + 1 < 10 ? QS : 1.f));
          const float kA = rstd * (tyB ? 0.f : (3 * ci < 10 ? QS : 1.f)), kB = tyB ? s1 : 0.f;
          const int lo = tyB ? 0 : (int)0x80000000;
#pragma unroll
          for (int n = 0; n < 4; ++n) {
            const f32x16 a = acc[n ^ 2];
#pragma unroll
            for (int r = 0; r < 4; ++r) {
              const f32x2 g = {relu(a[2 * r], lo), relu(a[2 * r + 1], lo)}, u = {a[2 * r + 8], a[2 * r + 9]};
              const f32x2 h = g * __builtin_elementwise_fma(u, (f32x2){kB, kB}, (f32x2){kA, kA});
              acc[2 + (n >> 1)][8 * (n & 1) + r] = __uint_as_float(pk2(h[0], h[1]));
            }
            SBAR();
#pragma unroll
            for (int r = 0; r < 4; ++r) {
              const f32x2 y = (f32x2){a[2 * r + 8], a[2 * r + 9]} * s1;
              acc[2 + (n >> 1)][8 * (n & 1) + 4 + r] = __uint_as_float(pk2(y[0], y[1]));
            }
            PIN(acc[2 + (n >> 1)]);
            SBAR();
          }
        } else {
          __builtin_amdgcn_s_setprio(3);
          const float sc = pass == 1 ? rstd * (ub < 10 ? QS : 1.f) : rstd * rstd;
          const unsigned vq = (pass == 2 || ub < 30) ? vo_st : OOB;
          const unsigned so = pass == 1 ? WS_QKV + tb * QKV_TB + (ub < 30 ? ub : 0) * 8192 : WS_A2 + tb * A2_MT + pg * 8192;
#pragma unroll
          for (int j = 0; j < 4; ++j) {
            u32x4 v;
#pragma unroll
            for (int r = 0; r < 4; ++r) {
              f32x2 h;
              if constexpr (pass == 1) h = (f32x2){acc[j >> 1][8 * (j & 1) + 2 * r], acc[j >> 1][8 * (j & 1) + 2 * r + 1]} * sc;
              else h = (f32x2){relu(acc[j ^ 2][2 * r], 0), relu(acc[j ^ 2][2 * r + 1], 0)} * ((f32x2){acc[j ^ 2][2 * r + 8], acc[j ^ 2][2 * r + 9]} * sc);
              v[r] = pk2(h[0], h[1]);
            }
            st16c<0>(v, vq, srd_ws, so + (pass == 1 ? j : j ^ 2) * 2048);
            SBAR();
          }
          __builtin_amdgcn_s_setprio(pass == 1 ? 0 : 1);
        }
      };
      const unsigned so_al = tyB ? WS_A2 + tb * A2_MT + ci * 8192 : WS_QKV + tb * QKV_TB + 3 * ci * 8192;
      u32x4 srd_hi = srd_ws;
      srd_hi[2] = tyB ? 0u : srd_ws[2];
      auto trickle = [&](auto J_) {
        constexpr int j = J_;
        st16p<((j >> 1) ^ 2) * 2048 + (j & 1) * 8192>(piece4<j & 3>(acc[2 + (j >> 2)]), vo_st, (j & 1) ? srd_hi : srd_ws, so_al);
      };
      auto att_setup = [&](auto J_) {
        constexpr int j = J_;
        const int lane = fresh_lane(), l31 = lane & 31, hh = lane >> 5;
        const unsigned tk = L0 + w * 1024 + l31 * 16;
        if constexpr (j == 0) { ak0 = tk + hh * AROW; PIN(ak0); }
        if constexpr (j == 1) { ak1 = tk + (hh ? L_FR : L_K2); PIN(ak1); }
        if constexpr (j == 2) { aq = tk + L_Q + hh * 4096; PIN(aq); }
        if constexpr (j == 3) { av = (lane & 16) ? 4 : ((lane & 1) << 1) | ((lane >> 1) & 1); PIN(av); }
        if constexpr (j == 4) { av += par; av = (3 + (av >> 1)) * AROW + 8 * (av & 1); PIN(av); }
        if constexpr (j == 5) { av = (lane & 18) == 18 ? L_FR + 8 : av; PIN(av); }
        if constexpr (j == 6) { av += L0 + w * 1024 + (4 * hh + ((lane & 15) >> 2)) * 16; PIN(av); }
      };
      float big = -1e30f;
      auto mask_setup = [&](auto R_) {
        constexpr int r = R_;
        if constexpr (r == 0) PIN(big);
        const bool far = __builtin_amdgcn_inverse_ballot_w64(edge_mask(r));
        mlo[r] = far ? 0.f : big; mhi[r] = far ? big : 0.f;
        PIN(mlo); PIN(mhi);
      };
      static_for<0, D - 1>(rd);
      STAMP(1);
      auto slot = [&](auto Z_) {
        constexpr int z = Z_, f = z2f(z);
        if constexpr (z + D - 1 < 412) rd(IC<z + D - 1>{});
        SBAR();
        if constexpr (f < 0) {
          constexpr int t = (z > 200 && z < 300) ? zct(z) & 1 : zct(z) ^ 2;
          if constexpr (zct(z) == 0) { rstd = __uint_as_float(mf[1]); mf[1] = mf[0] & 0xffffu; }
          const u32x4 e = __builtin_bit_cast(u32x4, wf[z % D]);
          acc[t] = __builtin_amdgcn_mfma_f32_32x32x8bf16_1k(__builtin_bit_cast(s16x4, (u32x2){e[0], e[1]}), __builtin_bit_cast(s16x4, mf), acc[t], 0, 0, 0);
        } else {
          constexpr int wu = f >> 4, i = f & 15;
          constexpr bool beta = wu >= 10 && wu < 15;
          constexpr int t = beta ? (i & 1) : (i & 3);
          constexpr int xi = wu < 10 ? 4 * wu + (i >> 2) : (beta ? 8 * (wu - 10) + (i >> 1) : 4 * (wu - 15) + (i >> 2));
          if constexpr (xi == 0) acc[t] = mfma32(wf[z % D], xf[xi], (f32x16){});
          else acc[t] = mfma32(wf[z % D], xf[xi], acc[t]);
        }
        SBAR();
        if constexpr (z == 10) { stat_load(); SBAR(); }
        if constexpr (z == 163) { STAMP(44); epi(IC<0>{}); STAMP(2); SBAR(); }
        if constexpr (z >= 166 && z < 166 + 8 * 9 && (z - 166) % 9 == 0) { trickle(IC<(z - 166) / 9>{}); SBAR(); }
        if constexpr (z == 247) { STAMP(45); epi(IC<1>{}); STAMP(3); SBAR(); }
        if constexpr (z == 247 + ZF1) {
          STAMP(47);
          wait_vm<0>();
          __builtin_amdgcn_s_setprio(3); put_flag(srd_ws, FL_1 + (tb * 32 + 2 * c + (w & 1)) * 8, tag); __builtin_amdgcn_s_setprio(0);
          STAMP(48);
          SBAR();
        }
        if constexpr (z >= 350 && z < 382 && !(z & 1)) { mask_setup(IC<(z - 350) / 2>{}); SBAR(); }
        if constexpr (z >= 382 && z < 403 && z % 3 == 1) { att_setup(IC<(z - 382) / 3>{}); SBAR(); }
        if constexpr (z == 411) { STAMP(46); epi(IC<2>{}); STAMP(4); SBAR(); }
      };
      static_for<0, 206>(slot);
      static_for<206, 412>(slot);
    }
    bar();
    STAMP(5);
    {
      u32x4 q0[2], q1[2];
      f32x16 o[2];
      {
        f32x16 S[2];
        bf16x8 kf[2], kg[2];
        s16x4 vf[4];
        u32x4 pa[2], pb[2];
        q0[0] = *(const LAS u32x4*)aq; kg[0] = lds_b128(ak0);
        q1[0] = *(const LAS u32x4*)(aq + 8192); kg[1] = lds_b128(ak1);
        q0[1] = *(const LAS u32x4*)(aq + 512); kf[0] = lds_b128(ak0 + 512);
        q1[1] = *(const LAS u32x4*)(aq + 8192 + 512); kf[1] = lds_b128(ak1 + 512);
        SBAR();
        S[0] = mfma32(kg[0], asbf(q0[0]), mlo);
        S[0] = mfma32(kg[1], asbf(q1[0]), S[0]);
        SBAR();
        auto grp = [&](auto K_, auto G_) {
          constexpr int k = K_, gq = G_;
          float e[4];
          static_for<0, 4>([&](auto J_) {
            constexpr int j = J_;
            e[j] = __builtin_amdgcn_exp2f(S[k & 1][4 * gq + j]);
            SBAR();
            if constexpr (gq == 1 && j < 2 && (k & 1) && k < 16) kf[j] = lds_b128((j ? ak1 : ak0) + ((k + 3) >> 1) * 512);
            if constexpr (gq == 3 && ((k & 1) || k == 0)) vf[j] = lds_tr(av + ((k + 1) >> 1) * 512 + j * 128);
            SBAR();
          });
          u32x4& d = gq < 2 ? pa[k & 1] : pb[k & 1];
          d[2 * (gq & 1)] = pk2(e[0], e[1]); d[2 * (gq & 1) + 1] = pk2(e[2], e[3]);
          PIN(d);
          SBAR();
        };
        static_for<0, 19>([&](auto K_) {
          constexpr int k = K_, n = k + 1, pv = k - 1;
          if constexpr (k == KF2) { wait_vm<0>(); put_flag(srd_ws, FL_2 + (tb * 32 + 2 * c + (w & 1)) * 8, tag); SBAR(); }
          if constexpr (FINE == 3 && (k & 1) == 0 && k < 18) STAMP(8 + k);
          f32x16 sn;
          if constexpr (n < 18) { sn = mfma32(kf[0], asbf(q0[n & 1]), n == 1 ? mlo : (n >= 16 ? mhi : (f32x16){})); PIN(sn); SBAR(); }
          if constexpr (k < 18) grp(K_, IC<0>{});
          if constexpr (n < 18) { sn = mfma32(kf[1], asbf(q1[n & 1]), sn); PIN(sn); SBAR(); }
          if constexpr (k < 18) grp(K_, IC<1>{});
          if constexpr (pv >= 0) { o[pv & 1] = mfma32(join(vf[0], vf[1]), asbf(pa[pv & 1]), pv < 2 ? (f32x16){} : o[pv & 1]); PIN(o[pv & 1]); SBAR(); }
          if constexpr (k < 18) grp(K_, IC<2>{});
          if constexpr (pv >= 0) { o[pv & 1] = mfma32(join(vf[2], vf[3]), asbf(pb[pv & 1]), o[pv & 1]); PIN(o[pv & 1]); SBAR(); }
          if constexpr (k < 18) grp(K_, IC<3>{});
          if constexpr (n < 18) S[n & 1] = sn;
        });
      }
      if constexpr (FINE == 3) STAMP(26);
      const int lane = fresh_lane(), l31 = lane & 31, hh = lane >> 5;
      if (!__all(o[0][12] < 1e18f && o[1][12] < 1e18f && o[0][12] > 1e-18f && o[1][12] > 1e-18f)) {
#pragma unroll
        for (int a = 0; a < 2; ++a) {
          q1[a][0] = hh ? q1[a][0] & 0xffffu : q1[a][0];
          float mx = -__builtin_inff();
          o[a] = (f32x16){};
#pragma unroll 1
          for (int it = 0; it < 18; ++it) {
            const int i = it < 9 ? it : it - 9;
            f32x16 t = mfma32(lds_b128(ak0 + (a + i) * 512), asbf(q0[a]), (f32x16){});
            t = mfma32(lds_b128(ak1 + (a + i) * 512), asbf(q1[a]), t);
#pragma unroll
            for (int r = 0; r < 16; ++r) {
              const unsigned dist = 256 + l31 - 32 * i - ((r & 3) + 8 * (r >> 2) + 4 * hh);
              t[r] = dist < 256u ? t[r] : -__builtin_inff();
            }
            if (it < 9) {
#pragma unroll
              for (int r = 0; r < 16; r += 2) mx = max3(mx, t[r], t[r + 1]);
              if (it == 8) mx = swap_max32(mx);
            } else {
              u32x4 p0, p1;
#pragma unroll
              for (int r = 0; r < 4; ++r) {
                p0[r] = pk2(__builtin_amdgcn_exp2f(t[2 * r] - mx), __builtin_amdgcn_exp2f(t[2 * r + 1] - mx));
                p1[r] = pk2(__builtin_amdgcn_exp2f(t[8 + 2 * r] - mx), __builtin_amdgcn_exp2f(t[9 + 2 * r] - mx));
              }
              const unsigned vb = av + (a + i) * 512;
              o[a] = mfma32(join(lds_tr(vb), lds_tr(vb + 128)), asbf(p0), o[a]);
              o[a] = mfma32(join(lds_tr(vb + 256), lds_tr(vb + 384)), asbf(p1), o[a]);
            }
          }
        }
      }
      __builtin_amdgcn_s_setprio(3);
      const unsigned so = WS_A2 + (4 * qb + w) * A2_MT;
#pragma unroll
      for (int a = 0; a < 2; ++a) {
        const float il = __builtin_amdgcn_rcpf(o[a][12]);
        st16c<0>((u32x4){pk2(o[a][0] * il, o[a][1] * il), pk2(o[a][2] * il, o[a][3] * il), pk2(o[a][4] * il, o[a][5] * il), pk2(o[a][6] * il, o[a][7] * il)},
                 hh * 1024 + l31 * 16, srd_ws, so + (192 + 2 * head) * 1024 + a * 512);
        st8c<0>((u32x2){pk2(o[a][8] * il, o[a][9] * il), pk2(o[a][10] * il, o[a][11] * il)}, hh ? OOB : l31 * 8, srd_ws,
                so + 256 * 1024 + head * 512 + a * 256);
      }
    }
    STAMP(6);
    {
      const int lane = fresh_lane();
      const unsigned d_a = L0 + w * 4096, so_a = WS_A2 + mt * A2_MT + w * 4096, l16 = lane * 16;
      auto adma = [&](auto R_) { constexpr int r = R_; dma16c_x4<a_slot(r)>(d_a, l16, srd_ws, so_a + rnd(r) * 16384); };
      wait_vm<0>();
      put_flag(srd_ws, FL_3 + ((4 * qb + w) * 32 + head) * 8, tag);
      STAMP(35);
      bar();
      __builtin_amdgcn_s_setprio(1);
      STAMP(36);
      static_for<9, 12>(adma);
      STAMP(37);
      wait_bar<0>();
      STAMP(38);
      __builtin_amdgcn_s_setprio(3);
      poll(srd_ws, l16 & 255, jn * FL_REP + FL_3 + mt * 256, tag);
      STAMP(39);
      static_for<12, 17>(adma);
      const int id = 64 * w + lane;
      ld16<0>(xres[0], (id / 10) * 1280 + (id % 10) * 16, srd_x, so_out);
      ld16<0>(xres[1], w < 2 ? ((id + 512) / 10) * 1280 + ((id + 512) % 10) * 16 : OOB, srd_x, so_out);
      STAMP(40);
      wait_bar<18>(); wait_bar<14>(); wait_bar<10>(); wait_bar<2>();
      wait_vm<0>();
      PIN(xres[0]); PIN(xres[1]);
      STAMP(7);
    }
  } else {
    ROLE_SETUP();
    __builtin_amdgcn_s_setprio(2);
    const int s = w - 4;
    const int lane = fresh_lane();
    const int rho = lane >> 3;
    unsigned vo_x = (32 * (rho >> 1) + (rho & 1)) * 1280 + (lane & 7) * 16;
    unsigned vo_q = ((rho & 1) + 4 * (rho >> 1)) * 3840 + (lane & 7) * 16;
    unsigned vo_g = ((rho & 1) + 4 * (rho >> 1)) * 3072 + (lane & 7) * 16;
    unsigned st_al = 3840 - 768 * tyB;
    unsigned vo_al[4];
#pragma unroll
    for (int m = 0; m < 4; ++m) { vo_al[m] = (tyB ? vo_g : vo_q) + m * 16 * st_al; PIN(vo_al[m]); }
    unsigned c_x = (p * 128 + 8 * s) * 1280, c_al = (ci + (1 - tyB) * (2 * ci + (s >> 1))) * 128 + 2 * (s & 1) * st_al;
    unsigned d_x = L0 + s * 4160, d_2 = L0 + s * 4128;
    int rot = ((10 * (c & 3) + 5 * (p & 7)) >> 2) % 10;
    PIN(vo_x); PINS(c_x); PINS(c_al); PINS(d_x); PINS(d_2); PINS(rot); PINS(st_al);
    SBAR();
    ARGS_READY();
    const u32x4 srd_x = SRD(ka, 0), srd_ws = SRD(kb, 0);
    const u32x4 srd_gu = (s >> 1) ? SRD(ka, 3) : SRD(ka, 2);
    const u32x4 srd_al = tyB ? srd_gu : SRD(ka, 1);
    srd_o = SRD(kb, 3);
    u32x4 srd_be, srd_rp;
    unsigned c_be, c_ga, d_1, vo_rp, so_rp[4], vo_cs, so_cs;
    auto kst = [&](int n) INL { int k = n + rot; PINS(k); return k >= 10 ? k - 10 : k; };
    unsigned so;
    auto piece = [&](auto Q_) {
      constexpr int k = Q_ >> 2, m = Q_ & 3;
      constexpr unsigned d = upos(k);
      if constexpr (k < 20 && !(k & 1)) {
        if constexpr (m == 0) so = c_x + kst(k >> 1) * 128;
        dma16<d + m * 1040, m * 2560>(d_x, vo_x, srd_x, so);
      } else if constexpr (k < 20) {
        if constexpr (m == 0) so = c_al + kst(k >> 1) * 64 * st_al;
        dma16<d + m * 1024, 0>(d_2, vo_al[m], srd_al, so);
      } else if constexpr (k == U_RP) {
        dma16<d + m * 1024, 0>(d_2, vo_rp, srd_rp, so_rp[m]);
      } else if constexpr (k == U_CS) {
        dma16<d + m * 1024, m * 16 * 3072>(d_2, vo_cs, srd_gu, so_cs);
      } else if constexpr (k < 25) {
        if constexpr (m == 0) so = c_be + kst(2 * (k - 20) + (s >> 1)) * (64 * 3840);
        dma16<d + m * 1024, m * 16 * 3840>(d_1, vo_q, srd_be, so);
      } else {
        if constexpr (m == 0) so = c_ga + kst(k < 27 ? k - 25 : k - 27) * (64 * 3072);
        dma16<d + m * 1024, m * 16 * 3072>(d_2, vo_g, srd_gu, so);
      }
    };
    STAMP(60);
    static_for<0, 4 * PRO>(piece);
    u32x2 ep;
    ld8c<FL_EPOCH>(ep, 0u, srd_ws, 0u);
    SBAR();

    so_out = mt * (64 * 1280) + jn * 160;
    srd_be = SRD(ka, 1); srd_be[2] = ub < 30 ? srd_be[2] : 0u;
    c_be = (ub < 30 ? ub : 0) * 128 + 2 * (s & 1) * 3840; c_ga = pg * 128 + 2 * (s & 1) * 3072;
    d_1 = L0 + (s & 1) * 8256 + (s >> 1) * 4096;
    const int ract = (s < 3) & (b * 3 + s < 680);
    const int ru = ract * (b * 3 + s), rs = ru / 10, rbl = ru - 10 * rs;
    {
      const int dn = rs < 48, tl = rs >= 64;
      srd_rp = dn ? SRD(kb, 1) : SRD(kb, 2);
      vo_rp = ract ? (tl ? 20 * (rho >> 2) + (rho & 3) : rho) * 1280 + (lane & 7) * 16 : OOB;
      const int r0 = rs * (40 - 8 * dn + 120 * tl) - 1920 * (1 - dn) - 8304 * tl;
      const int d1 = 8 + 32 * tl, d2 = 20 - 4 * dn + 60 * tl;
#pragma unroll
      for (int m = 0; m < 4; ++m) { so_rp[m] = (r0 + (m & 1) * d1 + (m >> 1) * d2) * 1280 + rbl * 128; PINS(so_rp[m]); }
      PIN(vo_rp); PINS(srd_rp);
    }
    vo_cs = p < 10 ? vo_g : OOB; so_cs = c_ga + (p < 10 ? p : 0) * (64 * 3072);
    const unsigned l16 = lane * 16, d_s = L0 + s * 1024;
    const int l31 = lane & 31, hh = lane >> 5;
    const unsigned selw = ((lane & 15) >> 2) == (lane >> 4) ? 0x3f803f80u : 0u;
    const bf16x8 sel = asbf((u32x4){selw, selw, selw, selw});
    const unsigned lcs = ((lane & 15) >> 2) * 128 + (lane >> 4) * 32 + 8 * (lane & 3);
    auto put_e = [&](bool beta, int col, float v) INL {
      const int gq = (col >> 2) & 3, n = 4 * (((gq & 1) << 1) | (gq >> 1)) + (col & 3);
      const int e = beta ? ((col >> 5) + 2 * (s >> 1)) * 64 + (col & 16) + n : (col >> 4) * 64 + 16 * (s >> 1) + n;
      *(LAS u32x2*)(L0 + L_E + (e + 32 * (s & 1)) * 8) = split3(v);
    };
    f32x4 cs[4] = {};
    bf16x8 cf[4];
    auto cs_read = [&](auto K_) {
      constexpr int k = K_;
      unsigned ac = (k >= 20 ? d_1 : d_2) + upos(k) + lcs; PIN(ac);
#pragma unroll
      for (int i = 0; i < 4; ++i) cf[i] = join(lds_tr(ac + i * 1024), lds_tr(ac + i * 1024 + 512));
    };
    auto cs_add = [&]() INL {
      if constexpr (!(ABL & 1)) {
#pragma unroll
        for (int i = 0; i < 4; ++i) cs[i % CSV] = mfma16(sel, cf[i], cs[i % CSV]);
#pragma unroll
        for (int i = 0; i < CSV; ++i) PIN(cs[i]);
      }
      SBAR();
    };
    auto cs_take = [&]() INL {
      float v = cs[0][0];
#pragma unroll
      for (int i = 1; i < CSV; ++i) v += cs[i][0];
#pragma unroll
      for (int i = 0; i < CSV; ++i) cs[i] = (f32x4){};
      return v + 0.f;
    };
    u32x4 stg;
    u64 tag = 0;
    auto open = [&](auto I_) {
      constexpr int I = I_;
      if constexpr (FINE == 1) STAMP(8 + I);
      wait_all<I == 17 ? 0 : younger_unit(need_unit(I), I)>();
      if constexpr (I < 15) cs_read(IC<wunit(I)>{});
      bar();
      if constexpr (FINE == 1) STAMP(35 + I);
      SBAR();
    };
    auto fills = [&](auto I_, auto A_, auto B_) {
      constexpr int q0 = 4 * fill_beg(I_), nq = 4 * fill_end(I_) - q0;
      static_for<imin(nq, A_), imin(nq, B_)>([&](auto J_) { piece(IC<q0 + J_>{}); });
      SBAR();
    };
    static_for<0, 8>([&](auto I_) {
      open(I_);
      fills(I_, IC<0>{}, IC<1>{});
      cs_add();
      fills(I_, IC<1>{}, IC<99>{});
      if constexpr (I_ == 5) { PIN(ep); tag = uni64(ep) + 1; }
      if constexpr (I_ == I_ST) ld16c<0>(stg, l31 * 16, srd_ws, WS_STAT + (p * 128 + 32 * s) * 16);
    });
    cs_read(IC<17>{});
    cs_add();
    open(IC<9>{});
    cs_add();
    fills(IC<9>{}, IC<0>{}, IC<99>{});
    put_e(false, lane, cs_take());
    {
      wait_vm<8>();
      PIN(stg);
      const unsigned tl = (unsigned)tag << 16, th = (unsigned)(tag >> 16);
#pragma unroll 1
      for (unsigned spin = 0; spin < SPIN_CAP; ++spin) {
        if (__all(((stg[0] ^ tl) | (stg[2] ^ tl)) < 65536u && stg[1] == th && stg[3] == th)) break;
        __builtin_amdgcn_s_sleep(1);
        ld16c<0>(stg, l31 * 16, srd_ws, WS_STAT + (p * 128 + 32 * s) * 16);
        wait_vm<0>();
        PIN(stg);
      }
      *(LAS u32x2*)(L0 + L_ST + (32 * s + l31) * 8) = (u32x2){(stg[0] & 0xffffu) | (stg[0] << 16), __float_as_uint(__builtin_amdgcn_rcpf(__uint_as_float(stg[2] << 16)))};
    }
    open(IC<10>{});
    fills(IC<10>{}, IC<0>{}, IC<4>{});
    cs_add();
    cs_read(IC<21>{});
    fills(IC<10>{}, IC<4>{}, IC<8>{});
    cs_add();
    fills(IC<10>{}, IC<8>{}, IC<99>{});
    open(IC<12>{});
    fills(IC<12>{}, IC<0>{}, IC<4>{});
    cs_add();
    cs_read(IC<23>{});
    fills(IC<12>{}, IC<4>{}, IC<8>{});
    cs_add();
    cs_read(IC<24>{});
    cs_add();
    put_e(true, lane, cs_take());
    fills(IC<12>{}, IC<8>{}, IC<99>{});
    {
      wait_vm<4>();
      f32x4 cg = {};
#pragma unroll
      for (int i = 0; i < 4; ++i) cg = mfma16(sel, join(lds_tr(d_2 + upos(U_CS) + lcs + i * 1024), lds_tr(d_2 + upos(U_CS) + lcs + i * 1024 + 512)), cg);
      const unsigned ar = d_2 + upos(U_RP) + (8 * (lane >> 4) + ((lane & 15) >> 2)) * 128 + 8 * (lane & 3);
      const unsigned vs = ract ? l16 : OOB;
#pragma unroll
      for (int t = 0; t < 4; ++t) {
        const int ct = 4 * rbl + t, cj = (ct * 13) >> 6;
        const bf16x8 f = join(lds_tr(ar + 32 * t), lds_tr(ar + 32 * t + 512));
        st16c<0>(__builtin_bit_cast(u32x4, f), vs, srd_ws, WS_R + ((cj * 68 + rs) * 5 + ct - 5 * cj) * 1024);
      }
      st4c<0>(__float_as_uint(cg[0] + 0.f), p < 10 ? lane * 4 : OOB, srd_ws, WS_CS + (4 * c + s) * 2560 + (p < 10 ? p : 0) * 256);
    }
    open(IC<15>{});
    fills(IC<15>{}, IC<0>{}, IC<99>{});
    open(IC<17>{});
    put_flag(srd_ws, FL_0 + b * 8, tag, s == 0 ? 8 : 0);
    fills(IC<17>{}, IC<0>{}, IC<99>{});

    u32x4 br[85], ar[16];
    const unsigned so_b = WS_R + jn * R_J + s * 5120;
    unsigned sob;
    auto bload = [&](auto I_) {
      constexpr int i = I_;
      if constexpr (i % 5 == 0) sob = so_b + rnd(i / 5) * 20480;
      if constexpr (i % 5 == 4) { sob += 4096; PINS(sob); }
      ld16c<(i % 5 < 4 ? (i % 5) * 1024 : 0)>(br[i], l16, srd_ws, sob);
    };
    const unsigned d_a = L0 + s * 4096, so_a = WS_A2 + mt * A2_MT + s * 4096;
    unsigned aa = (lane >> 4) * 1024 + (lane & 15) * 16;
    PIN(aa);
    unsigned soa;
    auto aload = [&](auto I_) {
      constexpr int i = I_;
      if constexpr ((i & 3) == 0) soa = so_a + rnd(i >> 2) * 16384;
      ld16c<(i & 3) * 256>(ar[i], aa, srd_ws, soa);
    };
    {
      open(IC<19>{});
      fills(IC<19>{}, IC<0>{}, IC<99>{});
      {
        const int tq = 4 * qb + s, sel3 = lane >> 4;
        poll(srd_ws, (sel3 == 0 ? (qb ? tq - 4 : tq) : (sel3 == 1 ? tq : mt)) * 256 + (l16 & 255), xcd * FL_REP + FL_1, tag);
      }
      STAMP(1);
      const unsigned s1o = WS_QKV + (4 * qb + s) * QKV_TB + o0 * 1024, s0o = qb ? s1o - 4 * QKV_TB : s1o;
      u32x4 f0a, f0b;
      ld16c<FL_0>(f0a, l16, srd_ws, xcd * FL_REP); ld16c<FL_0 + 1024>(f0b, l16, srd_ws, xcd * FL_REP);
      static_for<0, 3>([&](auto J_) {
        constexpr int i = J_;
        dma16c<L_Q + i * 4096, i * 1024>(d_s, l16, srd_ws, s1o);
        dma16c<krow(i) + 4096, (80 + i) * 1024>(d_s, l16, srd_ws, s1o);
      });
      STAMP(26);
      bar();
      STAMP(56);
#pragma unroll
      for (int hb = 0; hb < 2; ++hb)
        *(LAS u32x4*)(L0 + L_FR + (256 * hb + 64 * s) * 16 + l16) = (u32x4){(qb == 0 && hb == 0) ? 0x3f803f80u : 0x3f800000u, 0x3f803f80u, 0x3f803f80u, 0x3f803f80u};
      wait_vm<6>();
      PIN(f0a); PIN(f0b);
      if (!__all(tagged(f0a, tag) && tagged(f0b, tag))) { poll(srd_ws, l16, xcd * FL_REP + FL_0, tag); poll(srd_ws, l16, xcd * FL_REP + FL_0 + 1024, tag); }
      u32x4 g0, g1, g2;
      ld16c<0>(g0, l16, srd_ws, WS_CS + (4 * c + s) * 2560);
      ld16c<1024>(g1, l16, srd_ws, WS_CS + (4 * c + s) * 2560);
      ld16c<2048>(g2, hh ? OOB : l16, srd_ws, WS_CS + (4 * c + s) * 2560);
      static_for<0, 3>([&](auto J_) { constexpr int i = J_; dma16c<(3 + i) * AROW + 4096, (160 + i) * 1024>(d_s, l16, srd_ws, s1o); });
      wait_vm<6>();
      STAMP(28);
      const unsigned aq = d_s + L_Q + l31 * 16, ak = d_s + 4096 + l31 * 16;
      u32x4 q0[2], q1[2];
      f32x16 t[2];
#pragma unroll
      for (int a = 0; a < 2; ++a) {
        q0[a] = *(const LAS u32x4*)(aq + hh * 4096 + a * 512); q1[a] = *(const LAS u32x4*)(aq + 8192 + a * 512);
        const bool z0 = par && !hh;
        q0[a][0] = z0 ? 0u : q0[a][0]; q0[a][1] = z0 ? 0u : q0[a][1];
        q1[a][0] = hh ? 0u : q1[a][0]; q1[a][1] = hh ? 0u : q1[a][1];
        q1[a][2] = z0 ? q1[a][2] : 0u; q1[a][3] = z0 ? q1[a][3] : 0u;
        t[a] = mfma32(lds_b128(ak + hh * AROW + a * 512), asbf(q0[a]), (f32x16){});
        t[a] = mfma32(lds_b128(ak + L_K2 + a * 512), asbf(q1[a]), t[a]);
        *(LAS u32x2*)(aq + hh * 4096 + a * 512) = (u32x2){q0[a][0], q0[a][1]};
      }
#pragma unroll
      for (int a = 0; a < 2; ++a) {
        float mx = __builtin_amdgcn_fmed3f(t[a][0], t[a][1], __builtin_inff()), my = max3(mx, t[a][2], t[a][3]);
#pragma unroll
        for (int r = 4; r < 16; r += 4) { mx = max3(mx, t[a][r], t[a][r + 1]); my = max3(my, t[a][r + 2], t[a][r + 3]); }
        const unsigned ref = pk2(-1e30f, -swap_max32(max3(mx, my, my)));
        q1[a][0] = hh ? ref : q1[a][0];
        *(LAS u32x4*)(aq + 8192 + hh * 4096 + a * 512) = q1[a];
      }
      SBAR();
      STAMP(29);
      static_for<0, 3>([&](auto J_) {
        constexpr int i = J_;
        dma16c<krow(i), (80 + i) * 1024>(d_s, l16, srd_ws, s0o);
        dma16c<(3 + i) * AROW, (160 + i) * 1024>(d_s, l16, srd_ws, s0o);
      });
      wait_vm<9>();
      PIN(g0); PIN(g1); PIN(g2);
      float v[4];
#pragma unroll
      for (int j = 0; j < 4; ++j) v[j] = (__uint_as_float(g0[j]) + __uint_as_float(g1[j])) + __uint_as_float(g2[j]);
      put_e(false, 4 * (lane & 15) + 2 * hh + ((lane >> 4) & 1), swap_add16(swap_add32(v[0], v[2]), swap_add32(v[1], v[3])));
      put_flag(srd_ws, FL_EPOCH, tag, b == 0 && s == 0 ? 1 : 0);
      static_for<0, 5>(bload);
      static_for<0, 4>(aload);
      STAMP(57);
      wait_bar<63>();
    }
    STAMP(2);
    wait_bar<9>();
    STAMP(3);
    __builtin_amdgcn_s_setprio(0);
    {
      auto adma = [&](auto R_) { constexpr int r = R_; dma16c_x4<a_slot(r)>(d_a, l16, srd_ws, so_a + rnd(r) * 16384); };
      static_for<5, 10>(bload);
      static_for<4, 8>(aload);
      STAMP(4);
      bf16x8 af[4];
      f32x4 acc[4][5];
      unsigned cur, ap;
      constexpr auto bend = [](int q) constexpr { return q < 2 ? 15 + 5 * q : (q <= 5 ? 10 * q + 10 - (q == 5 ? 3 : 0) : imin(85, 32 + 5 * q)); };
      auto aread = [&](auto R_, auto I_) {
        constexpr int r = R_, i = I_;
        if constexpr (r < 16) af[i] = lds_b128(cur + i * 256);
        else {
          const u32x2 lo = *(const LAS u32x2*)(cur + i * 128), hi = *(const LAS u32x2*)(cur + i * 128 + 512);
          af[i] = asbf((u32x4){lo[0], lo[1], hi[0], hi[1]});
        }
        SBAR();
      };
      auto step = [&](auto R_, auto Q_) {
        constexpr int r = R_, q = Q_;
        if constexpr (r >= 0 && r < 16) { PIN(aa); cur = aa + d_a + a_slot(r); PIN(cur); }
        if constexpr (r == 16) { const int ln = fresh_lane(); cur = d_a + a_slot(r) + (ln >> 4) * 1024 + (ln & 15) * 8; PIN(cur); }
        SBAR();
        static_for<0, 20>([&](auto M_) {
          constexpr int m = M_, i = m / 5, t = m % 5;
          if constexpr (q >= 0) {
            if constexpr (i == 0) PIN(br[5 * q + t]);
            if constexpr (q < 4 && t == 0) PIN(ar[4 * q + i]);
            acc[i][t] = mfma16(asbf(br[5 * q + t]), q < 4 ? asbf(ar[(4 * q + i) & 15]) : af[i], q == 0 ? (f32x4){} : acc[i][t]);
            SBAR();
            constexpr int nb = bend(q) - bend(q - 1), j = (nb > 5 || q < 2) ? ((m & 1) ? m >> 1 : 99) : ((m & 3) == 1 ? m >> 2 : 99);
            if constexpr (j < nb) { bload(IC<bend(q - 1) + j>{}); SBAR(); }
            else if constexpr (q < 2 && j < 9) { aload(IC<4 * q + 8 + j - 5>{}); SBAR(); }
            if constexpr (q == 16 && i > 0) { *(LAS f32x4*)(ap + 16 * (i - 1) * PROW + 64 * t) = acc[i - 1][t]; SBAR(); }
          }
          if constexpr (r >= 0 && t == 4) aread(R_, IC<i>{});
        });
      };
      static_for<0, 4>([&](auto Q_) {
        constexpr int q = Q_;
        wait_vm<q < 3 ? 9 : 10>();
        if constexpr (q == 0) STAMP(5);
        step(IC<-1>{}, Q_);
      });
      STAMP(6);
      poll(srd_ws, l16 & 255, jn * FL_REP + FL_2 + mt * 256, tag);
      STAMP(30);
      static_for<4, 9>(adma);
      STAMP(31);
      wait_vm<16>();
      step(IC<4>{}, IC<-1>{});
      static_for<5, 18>([&](auto R_) {
        constexpr int r = R_, q = r - 1;
        if constexpr (r == RT) { STAMP(32); bar(); __builtin_amdgcn_s_setprio(2); STAMP(58); }
        if constexpr (r == 9 || (r >= 12 && r < 16)) {
          if constexpr (FINE == 2) STAMP(8 + r);
          bar();
          if constexpr (FINE == 2) STAMP(35 + r);
        }
        if constexpr (r < 9) wait_vm<4 * (8 - r) + bend(q - 1) - bend(3)>();
        if constexpr (5 * q + 4 >= bend(3)) wait_vm<bend(q - 1) - 5 * q - 5>();
        if constexpr (r == 14) {
          const int id = 64 * w + fresh_lane();
          ld16<0>(xres[0], (id / 10) * 1280 + (id % 10) * 16, srd_x, so_out);
        }
        if constexpr (r == 17) {
          PIN(xres[0]); xres[1] = (u32x4){0u, 0u, 0u, 0u};
          const int ln = fresh_lane();
          ap = L0 + (s < 3 ? s * PCLS : L_P3) + (ln & 15) * PROW + 16 * (ln >> 4);
          PIN(ap);
        }
        step(IC<(r < 17 ? r : -1)>{}, IC<q>{});
      });
#pragma unroll
      for (int t = 0; t < 5; ++t) *(LAS f32x4*)(ap + 48 * PROW + 64 * t) = acc[3][t];
      STAMP(7);
    }
  }
  {
    const int lane = fresh_lane();
    unsigned ra[2], go[2];
#pragma unroll
    for (int it = 0; it < 2; ++it) {
      const int id = 64 * (it ? (w & 1) + 8 : w) + lane, row = id / 10, cc = id - 10 * row;
      ra[it] = L0 + row * PROW + cc * 32; go[it] = row * 1280 + cc * 16;
      PIN(ra[it]); PIN(go[it]);
    }
    wait_bar<0>();
    PIN(xres[0]); PIN(xres[1]);
    auto fin = [&](int it) INL {
      f32x4 lo = *(const LAS f32x4*)(ra[it] + L_P3), hi = *(const LAS f32x4*)(ra[it] + L_P3 + 16);
#pragma unroll
      for (int k = 0; k < 3; ++k) { lo += *(const LAS f32x4*)(ra[it] + k * PCLS); hi += *(const LAS f32x4*)(ra[it] + k * PCLS + 16); }
      const u32x4 xv = xres[it];
      u32x4 ov;
      ov[0] = pk2(lo[0] + __uint_as_float(xv[0] << 16), lo[1] + __uint_as_float(xv[0] & 0xffff0000u));
      ov[1] = pk2(lo[2] + __uint_as_float(xv[1] << 16), lo[3] + __uint_as_float(xv[1] & 0xffff0000u));
      ov[2] = pk2(hi[0] + __uint_as_float(xv[2] << 16), hi[1] + __uint_as_float(xv[2] & 0xffff0000u));
      ov[3] = pk2(hi[2] + __uint_as_float(xv[3] << 16), hi[3] + __uint_as_float(xv[3] & 0xffff0000u));
      return ov;
    };
    if (w < 2) {
      const u32x4 o0v = fin(0), o1v = fin(1);
      st16<0>(o0v, go[0], srd_o, so_out);
      st16<0>(o1v, go[1], srd_o, so_out);
    } else {
      st16<0>(fin(0), go[0], srd_o, so_out);
    }
  }
  asm volatile(".Lcode_end:");
#endif
}

extern "C" void kernel_launch(void* const* d_in, const int* in_sizes, int n_in, void* d_out, int out_size, void* d_ws, size_t ws_size,
                              hipStream_t stream) {
  const unsigned mode = 0x00020000u;
  Params P;
  P.x = {d_in[0], SEQ * DM * 2, mode};
  P.wqkv = {d_in[1], DM * 3 * DM * 2, mode};
  P.wo = {d_in[2], DM * DM * 2, mode};
  P.wg = {d_in[4], DM * FF * 2, mode};
  P.wu = {d_in[5], DM * FF * 2, mode};
  P.wd = {d_in[6], FF * DM * 2, mode};
  P.out = {d_out, SEQ * DM * 2, mode};
  P.ws = {d_ws, WS_STAMP, mode};
  fused<<<256, 512, 0, stream>>>(P);
}
```

```cpp
#define PROBE 0
#include <hip/hip_runtime.h>
#include <stdint.h>
#include <type_traits>
#include <utility>

#define STAMPS 0
#define FINE 0
#define ABL 0
#define PRO 4
#define NB1 24
#define ZF1 28
#define KF2 3
#define CSV 2
#define POLL_SLEEP 6
#define NB0 10
#define BCAP 34
#define O_SLEEP 30
#define NAPS 2
#define MAP48 0
#define ROT 0
#define EPI_NAP 0
#define CLK "s_memtime"
#define RSH 2
#define RT 5
#define NLAUNCH 1

typedef __bf16 bf16x8 __attribute__((ext_vector_type(8)));
typedef __bf16 bf16x2 __attribute__((ext_vector_type(2)));
typedef short s16x4 __attribute__((ext_vector_type(4)));
typedef float f32x16 __attribute__((ext_vector_type(16)));
typedef float f32x4 __attribute__((ext_vector_type(4)));
typedef float f32x2 __attribute__((ext_vector_type(2)));
typedef unsigned u32x4 __attribute__((ext_vector_type(4)));
typedef unsigned u32x2 __attribute__((ext_vector_type(2)));
typedef unsigned u32x16 __attribute__((ext_vector_type(16)));
typedef unsigned long long u64;
#define LAS __attribute__((address_space(3)))
#define GAS __attribute__((address_space(1)))
#define DEV __device__ __forceinline__
#define INL __attribute__((always_inline))
template <int V> using IC = std::integral_constant<int, V>;

constexpr int SEQ = 2048, DM = 640, FF = 1536;
constexpr unsigned OOB = 0x7ff00000u;
constexpr unsigned QKV_TB = 240 * 1024;
constexpr unsigned A2_MT = 272 * 1024;
constexpr unsigned R_J = 68 * 5 * 1024;
constexpr unsigned FL_EPOCH = 0, FL_0 = 2048, FL_1 = 8192, FL_2 = 16384, FL_3 = 24576, FL_REP = 32768;
constexpr unsigned WS_STAT = 8 * FL_REP, WS_CS = WS_STAT + 32768, CS_SET = 4 * 5120, WS_QKV = 1048576, WS_A2 = WS_QKV + 32 * QKV_TB, WS_R = WS_A2 + 32 * A2_MT, WS_STAMP = WS_R + 8 * R_J;

constexpr unsigned UNIT = 16640;
static_assert(WS_CS + 32 * CS_SET <= WS_QKV, "workspace map");
constexpr unsigned AROW = 8256;
constexpr unsigned L_FR = 2 * AROW, L_K2 = 8 * UNIT, L_Q = L_K2 + 8192;
constexpr unsigned krow(int i) { return i < 2 ? i * AROW : L_K2; }
constexpr unsigned L_E = 160768, L_ST = L_E + 2048;
constexpr unsigned L_SY = L_E - 256, SY_FULL = 0, SY_EV = 16, SY_DONE = 32, SY_CNT = 48;
static_assert(AROW + 8192 <= UNIT && L_Q + 16384 <= L_SY && L_SY >= 9 * UNIT, "attention image");
constexpr unsigned L_A0 = 6 * AROW;
constexpr unsigned a_slot(int r) { return r < 12 ? L_A0 + (r & 3) * 16384 : (r < 15 ? (r - 12) * 16384 : L_A0 + (r - 11) * 16384); }
constexpr unsigned PROW = 336, PCLS = 64 * PROW;
static_assert(L_A0 + 4 * 16384 <= L_K2 && a_slot(16) + 16384 <= L_SY && 4 * PCLS <= a_slot(15), "phase-2 image");

template <int I, class F, int... J> DEV void static_for_impl(F& f, std::integer_sequence<int, J...>) { (f(IC<I + J>{}), ...); }
template <int I, int N, class F> DEV void static_for(F&& f) { if constexpr (I < N) static_for_impl<I>(f, std::make_integer_sequence<int, N - I>{}); }

template <unsigned LOFF, unsigned SOFF> DEV void dma16(unsigned lb, unsigned voff, u32x4 srd, unsigned sb) {
  unsigned t;
  asm volatile("s_add_u32 m0, %1, %5\n\ts_add_u32 %0, %4, %6\n\tbuffer_load_dwordx4 %2, %3, %0 offen lds"
               : "=&s"(t) : "s"(lb), "v"(voff), "s"(srd), "s"(sb), "i"(LOFF), "i"(SOFF) : "memory", "scc");
}
template <unsigned LOFF, unsigned SOFF> DEV void dma16c(unsigned lb, unsigned voff, u32x4 srd, unsigned sb) {
  unsigned t;
  asm volatile("s_add_u32 m0, %1, %5\n\ts_add_u32 %0, %4, %6\n\tbuffer_load_dwordx4 %2, %3, %0 offen sc1 lds"
               : "=&s"(t) : "s"(lb), "v"(voff), "s"(srd), "s"(sb), "i"(LOFF), "i"(SOFF) : "memory", "scc");
}
template <unsigned LOFF> DEV void dma16c_x4(unsigned lb, unsigned voff, u32x4 srd, unsigned sb) {
  asm volatile("s_add_u32 m0, %0, %4\n\ts_nop 0\n\tbuffer_load_dwordx4 %1, %2, %3 offen sc1 lds\n\t"
               "buffer_load_dwordx4 %1, %2, %3 offen offset:1024 sc1 lds\n\tbuffer_load_dwordx4 %1, %2, %3 offen offset:2048 sc1 lds\n\t"
               "buffer_load_dwordx4 %1, %2, %3 offen offset:3072 sc1 lds"
               :: "s"(lb), "v"(voff), "s"(srd), "s"(sb), "i"(LOFF) : "memory", "scc");
}
template <int OFF> DEV void ld16c(u32x4& v, unsigned voff, u32x4 srd, unsigned soff) {
  asm volatile("buffer_load_dwordx4 %0, %1, %2, %3 offen offset:%4 sc1" : "=v"(v) : "v"(voff), "s"(srd), "s"(soff), "i"(OFF) : "memory");
}
template <int OFF> DEV void ld8c(u32x2& v, unsigned voff, u32x4 srd, unsigned soff) {
  asm volatile("buffer_load_dwordx2 %0, %1, %2, %3 offen offset:%4 sc1" : "=v"(v) : "v"(voff), "s"(srd), "s"(soff), "i"(OFF) : "memory");
}
template <int OFF> DEV void ld16(u32x4& v, unsigned voff, u32x4 srd, unsigned soff) {
  asm volatile("buffer_load_dwordx4 %0, %1, %2, %3 offen offset:%4" : "=v"(v) : "v"(voff), "s"(srd), "s"(soff), "i"(OFF) : "memory");
}
template <int OFF> DEV void st16c(u32x4 v, unsigned voff, u32x4 srd, unsigned soff) {
  asm volatile("buffer_store_dwordx4 %0, %1, %2, %3 offen offset:%4 sc1\n\ts_nop 1" ::"v"(v), "v"(voff), "s"(srd), "s"(soff), "i"(OFF) : "memory");
}
template <int OFF> DEV void st16(u32x4 v, unsigned voff, u32x4 srd, unsigned soff) {
  asm volatile("buffer_store_dwordx4 %0, %1, %2, %3 offen offset:%4\n\ts_nop 1" ::"v"(v), "v"(voff), "s"(srd), "s"(soff), "i"(OFF) : "memory");
}
template <unsigned SOFF> DEV void st16p(u32x4 v, unsigned voff, u32x4 srd, unsigned sb) {
  unsigned t;
  asm volatile("s_setprio 3\n\ts_add_u32 %0, %4, %5\n\tbuffer_store_dwordx4 %1, %2, %3, %0 offen sc1\n\ts_setprio 0\n\ts_nop 0"
               : "=&s"(t) : "v"(v), "v"(voff), "s"(srd), "s"(sb), "i"(SOFF) : "memory", "scc");
}
template <int OFF> DEV void st8c(u32x2 v, unsigned voff, u32x4 srd, unsigned soff) {
  asm volatile("buffer_store_dwordx2 %0, %1, %2, %3 offen offset:%4 sc1" ::"v"(v), "v"(voff), "s"(srd), "s"(soff), "i"(OFF) : "memory");
}
template <int OFF> DEV void st4c(unsigned v, unsigned voff, u32x4 srd, unsigned soff) {
  asm volatile("buffer_store_dword %0, %1, %2, %3 offen offset:%4 sc1" ::"v"(v), "v"(voff), "s"(srd), "s"(soff), "i"(OFF) : "memory");
}
template <int N> DEV void wait_vm() { asm volatile("s_waitcnt vmcnt(%0)" ::"i"(N > 63 ? 63 : N) : "memory"); }
template <int N> DEV void wait_all() { asm volatile("s_waitcnt vmcnt(%0) lgkmcnt(0)" ::"i"(N > 63 ? 63 : N) : "memory"); }
template <int N> DEV void wait_bar() { asm volatile("s_waitcnt vmcnt(%0) lgkmcnt(0)\n\ts_barrier" ::"i"(N > 63 ? 63 : N) : "memory"); }
DEV void bar() { asm volatile("s_barrier" ::: "memory"); }
#define PIN(x) asm volatile("" : "+v"(x))
#define PINS(x) asm volatile("" : "+s"(x))
#define SBAR() __builtin_amdgcn_sched_barrier(0)
DEV int fresh_lane() { int l; asm volatile("v_mbcnt_lo_u32_b32 %0, -1, 0\n\tv_mbcnt_hi_u32_b32 %0, -1, %0" : "=v"(l)); return l; }

DEV bf16x8 lds_b128(unsigned a) { return *(const LAS bf16x8*)a; }
DEV s16x4 lds_tr(unsigned a) { return __builtin_amdgcn_ds_read_tr16_b64_v4i16((LAS s16x4*)a); }
DEV bf16x8 join(s16x4 lo, s16x4 hi) { return __builtin_bit_cast(bf16x8, __builtin_shufflevector(lo, hi, 0, 1, 2, 3, 4, 5, 6, 7)); }
DEV bf16x8 asbf(u32x4 v) { return __builtin_bit_cast(bf16x8, v); }
DEV f32x16 mfma32(bf16x8 a, bf16x8 b, f32x16 c) { return __builtin_amdgcn_mfma_f32_32x32x16_bf16(a, b, c, 0, 0, 0); }
DEV f32x4 mfma16(bf16x8 a, bf16x8 b, f32x4 c) { return __builtin_amdgcn_mfma_f32_16x16x32_bf16(a, b, c, 0, 0, 0); }
DEV unsigned pk2(float lo, float hi) { return __builtin_bit_cast(unsigned, (bf16x2){(__bf16)lo, (__bf16)hi}); }
DEV float rbf(float x) { return (float)(__bf16)x; }
DEV float max3(float a, float b, float c) { float r; asm("v_max3_f32 %0, %1, %2, %3" : "=v"(r) : "v"(a), "v"(b), "v"(c)); return r; }
template <int J> DEV u32x4 piece4(f32x16 a) { return __builtin_bit_cast(u32x4, __builtin_shufflevector(a, a, 4 * J, 4 * J + 1, 4 * J + 2, 4 * J + 3)); }
DEV float relu(float x, int lo) { const int i = __float_as_int(x); return __int_as_float(i > lo ? i : lo); }
DEV u32x2 split3(float c) { const float a = rbf(c), b = rbf(c - a), d = c - a - b; return (u32x2){pk2(a, b), pk2(d, 0.f)}; }
template <int CTRL> DEV float dpp_add(float x) { return x + __int_as_float(__builtin_amdgcn_update_dpp(0, __float_as_int(x), CTRL, 0xf, 0xf, true)); }
DEV float swap_add32(float a, float b) { auto r = __builtin_amdgcn_permlane32_swap(__float_as_uint(a), __float_as_uint(b), false, false); return __uint_as_float(r[0]) + __uint_as_float(r[1]); }
DEV float swap_add16(float a, float b) { auto r = __builtin_amdgcn_permlane16_swap(__float_as_uint(a), __float_as_uint(b), false, false); return __uint_as_float(r[0]) + __uint_as_float(r[1]); }
DEV float swap_max32(float a) { auto r = __builtin_amdgcn_permlane32_swap(__float_as_uint(a), __float_as_uint(a), false, false); return max3(a, __uint_as_float(r[0]), __uint_as_float(r[1])); }
DEV u64 uni64(u32x2 v) { return ((u64)(unsigned)__builtin_amdgcn_readfirstlane(v[1]) << 32) | (unsigned)__builtin_amdgcn_readfirstlane(v[0]); }
constexpr u64 edge_mask(int r) { const int kk = (r & 3) + 8 * (r >> 2); return ((1ull << kk) - 1) | (((1ull << (kk + 4)) - 1) << 32); }

struct Buf { const void* p; unsigned bytes, mode; };
struct Params { Buf x, wqkv, wg, wu, ws, wd, wo, out; };

#define STAMP(i) do { if constexpr ((i) == PROBE && PROBE > 0) { SBAR(); asm volatile("s_memrealtime %0\n\ts_waitcnt lgkmcnt(0)" : "=s"(pr_t) :: "memory"); SBAR(); } } while (0)

constexpr unsigned SPIN_CAP = 200000;
DEV bool tagged(u32x4 v, u64 tag) { return (((v[0] ^ (unsigned)tag) | (v[2] ^ (unsigned)tag)) | ((v[1] ^ (unsigned)(tag >> 32)) | (v[3] ^ (unsigned)(tag >> 32)))) == 0u; }
DEV void poll(u32x4 ws, unsigned va, unsigned sa, u64 tag) {
  if (ABL & 4) { wait_vm<0>(); return; }
#pragma unroll 1
  for (unsigned spin = 0; spin < SPIN_CAP; ++spin) {
    u32x4 a;
    ld16c<0>(a, va, ws, sa);
    wait_vm<0>(); PIN(a);
    if (__all(tagged(a, tag))) break;
    __builtin_amdgcn_s_sleep(POLL_SLEEP);
  }
}
DEV void put_flag(u32x4 ws, unsigned off, u64 tag, int copies = 8) {
  const int lane = fresh_lane();
  st8c<0>((u32x2){(unsigned)tag, (unsigned)(tag >> 32)}, lane < copies ? lane * FL_REP : OOB, ws, off);
}

constexpr int U_RP = 22;
constexpr int imin(int a, int b) { return a < b ? a : b; }
constexpr int wunit(int I) { return I < 10 ? 2 * I + 1 : (I < 12 ? I + 10 : (I < 15 ? I + 12 : (I == 15 ? 23 : I + 11))); }
constexpr int windex(int u) { int I = 0; while (wunit(I) != (u < 20 ? u | 1 : u)) ++I; return I; }
constexpr int prev_unit(int j) { return j < 32 ? j - 9 : (j < 35 ? j - 8 : 23); }
constexpr unsigned upos(int k) { return k < 9 ? ((k + 8) % 9) * UNIT : upos(prev_unit(k)); }
static_assert(upos(27) == 8 * UNIT && upos(28) == 0 && upos(29) == UNIT && upos(30) == 2 * UNIT, "positions released first hold the attention image");
constexpr bool has_bar(int I) { return I <= 8 || I == 10 || I == 12 || I == 15; }
template <int K, int OFF, int NAP = 0> DEV void await(unsigned& v, unsigned addr) {
  unsigned n;
  asm volatile("v_cmp_ge_u32 vcc, %3, %0\n\ts_cbranch_vccz 2f\n\ts_mov_b32 %1, 0x200000\n1:\n\tds_read_b32 %0, %2 offset:%4\n\ts_sub_u32 %1, %1, 1\n\t"
               "s_waitcnt lgkmcnt(0)\n\ts_cbranch_scc1 2f\n\tv_cmp_ge_u32 vcc, %3, %0\n\ts_cbranch_vccz 2f\n\ts_sleep %5\n\ts_branch 1b\n2:"
               : "+v"(v), "=&s"(n) : "v"(addr), "i"(K), "i"(OFF), "i"(NAP) : "vcc", "scc", "memory");
}

#define ROLE_SETUP()                                                                                                                              \
  u32x16 ka, kb;                                                                                                                                  \
  asm volatile("s_load_dwordx16 %0, %2, 0x0\n\ts_load_dwordx16 %1, %2, 0x40" : "=&s"(ka), "=&s"(kb) : "s"(__builtin_amdgcn_kernarg_segment_ptr())); \
  int b = blockIdx.x; PINS(b);                                                                                                                    \
  const int xcd = b & 7, rr = b >> 3;                                                                                                             \
  const int p = (xcd >> 2) * 8 + (rr & 7), c = (xcd & 3) * 4 + (rr >> 3);                                                                         \
  const int tyB = c & 1, ci = c >> 1;                                                                                                             \
  const int ub = 3 * ci + 2 + tyB * (22 - 2 * ci), pg = 16 + ci - 8 * tyB;                                                                        \
  const int head = 4 * xcd + (rr & 3), qb = rr >> 2, par = head & 1, o0 = (5 * head) >> 1;                                                        \
  const int mt = MAP48 ? 4 * xcd + (rr & 3) : (xcd >> 1) * 8 + (rr & 7), jn = MAP48 ? rr >> 2 : (xcd & 1) * 4 + (rr >> 3);                                                                       \
  auto rnd = [&](int r) INL {                                                                                                                     \
    const int r4 = ROT == 0 ? 0 : (ROT == 1 ? jn + mt : (ROT == 2 ? (jn + mt) & 1 : mt)), r8 = ROT == 1 ? 2 * jn + mt : r4;  \
    int v = ROT == 2 ? (r < 16 ? r ^ r4 : r) : (r < 4 ? (r + r4) & 3 : (r < 12 ? 4 + ((r + r8) & 7) : (r < 16 ? 12 + ((r + r4) & 3) : 16)));                        \
    PINS(v);                                                                                                                                      \
    return v;                                                                                                                                     \
  }

__global__ __launch_bounds__(512, 2) void fused(Params P) {
#if defined(__HIP_DEVICE_COMPILE__)
  {
    unsigned c0, c1, len;
    asm volatile("s_getpc_b64 s[98:99]\n.Lpc_here:\n\ts_sub_u32 %0, s98, .Lpc_here-_Z5fused6Params\n\ts_subb_u32 %1, s99, 0\n\ts_mov_b32 %2, .Lcode_end-_Z5fused6Params"
                 : "=s"(c0), "=s"(c1), "=s"(len) :: "s98", "s99", "scc");
    const u32x4 srd_c = {c0, c1 & 0xffffu, len, 0x00020000u};
    asm volatile("buffer_load_dword v255, %0, %1, 0 offen" :: "v"(threadIdx.x * 2048 + (blockIdx.x & 120) * 16), "s"(srd_c) : "memory", "v255");
    SBAR();
  }
#define ARGS_READY() asm volatile("s_waitcnt lgkmcnt(0)" : "+s"(ka), "+s"(kb))
#define SRD(v, i) __builtin_shufflevector(v, v, 4 * (i), 4 * (i) + 1, 4 * (i) + 2, 4 * (i) + 3)
  __shared__ __attribute__((aligned(1024))) char smem[163840];
  const unsigned L0 = (unsigned)(uintptr_t)(LAS char*)smem;
  const int w = __builtin_amdgcn_readfirstlane(threadIdx.x >> 6);
  u64 pr_0; asm volatile("s_memrealtime %0\n\ts_waitcnt lgkmcnt(0)" : "=s"(pr_0) :: "memory");
  u64 pr_t = pr_0;
  u32x4 xres[2], srd_o;
  unsigned so_out;

  if (w < 4) {
    ROLE_SETUP();
    ARGS_READY();
    const u32x4 srd_ws = SRD(kb, 0), srd_x = SRD(ka, 0);
    srd_o = SRD(kb, 3);
    so_out = mt * (64 * 1280) + jn * 160;
    const int tb = 2 * p + (w >> 1);
    u64 tag;
    f32x16 acc[4];
    unsigned vo_st;
    unsigned ak0, ak1, av, aq, sy;
    f32x16 mlo, mhi;
    {
      constexpr int D = 5;
      bf16x8 xf[40], wf[D];
      unsigned ax, aw;
      {
        const int lane = fresh_lane(), l31 = lane & 31, hh = lane >> 5;
        ax = L0 + (l31 >> 1) * 1040 + (2 * w + (l31 & 1)) * 128 + hh * 16;
      }
      auto set_aw = [&](bool beta) INL {
        const int lane = fresh_lane(), hh = lane >> 5, blk = (lane >> 4) & 1, q = (lane & 15) >> 2, pp = lane & 3;
        const int pi = ((pp & 1) << 1) | (pp >> 1);
        aw = L0 + (q & 1) * 128 + hh * 512 + 8 * pi + (beta ? (q >> 1) * 8256 + 32 * blk : (2 * blk + (q >> 1)) * 4128);
        PIN(aw);
      };
      set_aw(false);
      unsigned cur, cx;
      unsigned fw;
      {
        const int lane = fresh_lane();
        *(LAS unsigned*)(L0 + L_SY + w * 64 + (lane & 15) * 4) = 0u;
        asm volatile("s_waitcnt lgkmcnt(0)" ::: "memory");
      }
      u32x2 mf;
      float rstd;
      u32x2 ep;
      u32x4 xv[3];
      const int srow = p * 128 + 8 * c + 2 * w;
      auto stat_load = [&]() INL {
        const int lane = fresh_lane(), l31 = lane & 31;
        const unsigned vo = (lane >> 5) * 1280 + l31 * 16;
        __builtin_amdgcn_s_setprio(3);
        ld8c<FL_EPOCH>(ep, 0u, srd_ws, 0u);
        ld16<0>(xv[0], vo, srd_x, srow * 1280);
        ld16<512>(xv[1], vo, srd_x, srow * 1280);
        ld16<1024>(xv[2], l31 < 16 ? vo : OOB, srd_x, srow * 1280);
        __builtin_amdgcn_s_setprio(0);
      };
      auto stat_put = [&]() INL {
        wait_vm<0>();
        PIN(ep); PIN(xv[0]); PIN(xv[1]); PIN(xv[2]);
        tag = uni64(ep) + 1;
        float s1 = 0.f, s2 = 0.f;
        const bf16x2 one2 = __builtin_bit_cast(bf16x2, 0x3f803f80u);
#pragma unroll
        for (int i = 0; i < 12; ++i) {
          const unsigned raw = xv[i >> 2][i & 3];
          const bf16x2 v = __builtin_bit_cast(bf16x2, raw);
          s1 = __builtin_amdgcn_fdot2_f32_bf16(v, one2, s1, false);
          s2 = __builtin_amdgcn_fdot2_f32_bf16(v, v, s2, false);
        }
        s1 = dpp_add<0xb1>(s1); s2 = dpp_add<0xb1>(s2);
        s1 = dpp_add<0x4e>(s1); s2 = dpp_add<0x4e>(s2);
        s1 = dpp_add<0x141>(s1); s2 = dpp_add<0x141>(s2);
        s1 = dpp_add<0x140>(s1); s2 = dpp_add<0x140>(s2);
        s1 = swap_add16(s1, s1); s2 = swap_add16(s2, s2);
        const float mean = s1 * (1.f / DM), var = fmaxf(s2 * (1.f / DM) - mean * mean, 0.f);
        const unsigned pay = pk2(-mean, __builtin_amdgcn_sqrtf(rbf(rbf(var) + 1e-5f)));
        const int lane = fresh_lane(), l31 = lane & 31;
        const u32x2 gr = {((unsigned)tag << 16) | ((lane & 1) ? pay >> 16 : pay & 0xffffu), (unsigned)(tag >> 16)};
        __builtin_amdgcn_s_setprio(3);
        st8c<0>(gr, l31 < 2 ? (lane >> 5) * 16 + l31 * 8 : OOB, srd_ws, WS_STAT + srow * 16);
        __builtin_amdgcn_s_setprio(0);
      };
      constexpr auto z2f = [](int z) constexpr { return z < 160 ? z : (z < 164 ? -1 : (z < 244 ? z - 4 : (z < 248 ? -1 : (z < 408 ? z - 8 : -1)))); };
      constexpr auto zct = [](int z) constexpr { return z < 164 ? z - 160 : (z < 248 ? z - 244 : z - 408); };
      auto rd = [&](auto Z_) {
        constexpr int z = Z_, f = z2f(z);
        if constexpr (f < 0) {
          constexpr int t = zct(z);
          if constexpr (t == 0) {
            const int lane = fresh_lane();
            cur = L0 + L_E + lane * 8; PIN(cur);
            cx = L0 + L_ST + w * 256 + (lane & 31) * 8; PIN(cx);
            if constexpr (z < 400) bar(); else await<0, SY_EV>(fw, sy);
            mf = *(const LAS u32x2*)cx;
          }
          if constexpr (t == 1 && z < 400) set_aw(z < 200);
          const u32x2 e = *(const LAS u32x2*)(cur + (z > 200 && z < 300 ? t : t ^ 2) * 512);
          wf[z % D] = asbf((u32x4){e[0], e[1], e[0], e[1]});
        } else {
          constexpr int wu = f >> 4, i = f & 15, k = wunit(wu);
          if constexpr (i == 0) {
            cur = aw + upos(k); PIN(cur);
            if constexpr (wu < 10) { cx = ax + upos(k - 1); PIN(cx); }
            if constexpr (wu == 2) { SBAR(); stat_put(); SBAR(); }
            if constexpr (has_bar(wu) && wu != 10 && wu != 15) bar();
            if constexpr (wu == 19 || wu == 23) await<(wu == 23), SY_FULL>(fw, sy);
          }
          if constexpr (i == D && (wu == 16 || wu == 18 || wu == 20)) *(LAS unsigned*)(sy + SY_DONE) = wu;
          if constexpr (wu < 10 && (i == 0 || (i & 3) == 2) && i < 12) xf[4 * wu + ((i + 2) >> 2)] = lds_b128(cx + 32 * ((i + 2) >> 2));
          constexpr bool beta = wu >= 10 && wu < 15;
          constexpr int off = beta ? (i >> 1) * 1024 + (i & 1) * 64 : (i >> 2) * 1024 + (i & 3) * 32;
          wf[z % D] = join(lds_tr(cur + off), lds_tr(cur + off + 256));
          if constexpr (i == 11 && (wu == 18 || wu == 22 || wu == 24)) fw = *(const LAS unsigned*)(sy + (wu == 24 ? SY_EV : SY_FULL));
          if constexpr (i == 2 && wu == 16) { sy = L0 + L_SY + w * 64 + (fresh_lane() & 3) * 4; PIN(sy); }
        }
      };
      auto epi = [&](auto PASS_) {
        constexpr int pass = PASS_;
        constexpr float QS = 0.22360679774997896f * 1.4426950408889634f;
        const int lane = fresh_lane();
        vo_st = (lane >> 5) * 1024 + (32 * (w & 1) + (lane & 31)) * 16;
        if constexpr (pass == 0) {
          const float s1 = rstd * (tyB ? rstd : (3 * ci + 1 < 10 ? QS : 1.f));
          const float kA = rstd * (tyB ? 0.f : (3 * ci < 10 ? QS : 1.f)), kB = tyB ? s1 : 0.f;
          const int lo = tyB ? 0 : (int)0x80000000;
#pragma unroll
          for (int n = 0; n < 4; ++n) {
            const f32x16 a = acc[n ^ 2];
#pragma unroll
            for (int r = 0; r < 4; ++r) {
              const f32x2 g = {relu(a[2 * r], lo), relu(a[2 * r + 1], lo)}, u = {a[2 * r + 8], a[2 * r + 9]};
              const f32x2 h = g * __builtin_elementwise_fma(u, (f32x2){kB, kB}, (f32x2){kA, kA});
              acc[2 + (n >> 1)][8 * (n & 1) + r] = __uint_as_float(pk2(h[0], h[1]));
            }
            SBAR();
#pragma unroll
            for (int r = 0; r < 4; ++r) {
              const f32x2 y = (f32x2){a[2 * r + 8], a[2 * r + 9]} * s1;
              acc[2 + (n >> 1)][8 * (n & 1) + 4 + r] = __uint_as_float(pk2(y[0], y[1]));
            }
            PIN(acc[2 + (n >> 1)]);
            SBAR();
          }
        } else {
          __builtin_amdgcn_s_setprio(3);
          const float sc = pass == 1 ? rstd * (ub < 10 ? QS : 1.f) : rstd * rstd;
          const unsigned vq = (pass == 2 || ub < 30) ? vo_st : OOB;
          const unsigned so = pass == 1 ? WS_QKV + tb * QKV_TB + (ub < 30 ? ub : 0) * 8192 : WS_A2 + tb * A2_MT + pg * 8192;
#pragma unroll
          for (int j = 0; j < 4; ++j) {
            u32x4 v;
#pragma unroll
            for (int r = 0; r < 4; ++r) {
              f32x2 h;
              if constexpr (pass == 1) h = (f32x2){acc[j >> 1][8 * (j & 1) + 2 * r], acc[j >> 1][8 * (j & 1) + 2 * r + 1]} * sc;
              else h = (f32x2){relu(acc[j ^ 2][2 * r], 0), relu(acc[j ^ 2][2 * r + 1], 0)} * ((f32x2){acc[j ^ 2][2 * r + 8], acc[j ^ 2][2 * r + 9]} * sc);
              v[r] = pk2(h[0], h[1]);
            }
            st16c<0>(v, vq, srd_ws, so + (pass == 1 ? j : j ^ 2) * 2048);
            SBAR();
          }
          __builtin_amdgcn_s_setprio(pass == 1 ? 0 : 1);
        }
      };
      const unsigned so_al = tyB ? WS_A2 + tb * A2_MT + ci * 8192 : WS_QKV + tb * QKV_TB + 3 * ci * 8192;
      u32x4 srd_hi = srd_ws;
      srd_hi[2] = tyB ? 0u : srd_ws[2];
      auto trickle = [&](auto J_) {
        constexpr int j = J_;
        st16p<((j >> 1) ^ 2) * 2048 + (j & 1) * 8192>(piece4<j & 3>(acc[2 + (j >> 2)]), vo_st, (j & 1) ? srd_hi : srd_ws, so_al);
      };
      auto att_setup = [&](auto J_) {
        constexpr int j = J_;
        const int lane = fresh_lane(), l31 = lane & 31, hh = lane >> 5;
        const unsigned tk = L0 + w * 1024 + l31 * 16;
        if constexpr (j == 0) { ak0 = tk + hh * AROW; PIN(ak0); }
        if constexpr (j == 1) { ak1 = tk + (hh ? L_FR : L_K2); PIN(ak1); }
        if constexpr (j == 2) { aq = tk + L_Q + hh * 4096; PIN(aq); }
        if constexpr (j == 3) { av = (lane & 16) ? 4 : ((lane & 1) << 1) | ((lane >> 1) & 1); PIN(av); }
        if constexpr (j == 4) { av += par; av = (3 + (av >> 1)) * AROW + 8 * (av & 1); PIN(av); }
        if constexpr (j == 5) { av = (lane & 18) == 18 ? L_FR + 8 : av; PIN(av); }
        if constexpr (j == 6) { av += L0 + w * 1024 + (4 * hh + ((lane & 15) >> 2)) * 16; PIN(av); }
      };
      float big = -1e30f, ml[16], mh[16];
      auto mask_setup = [&](auto R_) {
        constexpr int r = R_;
        if constexpr (r == 0) PIN(big);
        asm volatile("s_mov_b32 vcc_lo, %3\n\ts_mov_b32 vcc_hi, %4\n\tv_cndmask_b32 %1, 0, %2, vcc\n\tv_sub_f32 %0, %2, %1"
                     : "=v"(ml[r]), "=&v"(mh[r]) : "v"(big), "i"((unsigned)edge_mask(r)), "i"((unsigned)(edge_mask(r) >> 32)) : "vcc");
        if constexpr (r == 15) {
#pragma unroll
          for (int j = 0; j < 16; ++j) { mlo[j] = ml[j]; mhi[j] = mh[j]; }
        }
      };
      static_for<0, D - 1>(rd);
      STAMP(1);
      auto slot = [&](auto Z_) {
        constexpr int z = Z_, f = z2f(z);
        if constexpr (z + D - 1 < 412) rd(IC<z + D - 1>{});
        SBAR();
        if constexpr (f < 0) {
          constexpr int t = (z > 200 && z < 300) ? zct(z) & 1 : zct(z) ^ 2;
          if constexpr (zct(z) == 0) { rstd = __uint_as_float(mf[1]); mf[1] = mf[0] & 0xffffu; }
          const u32x4 e = __builtin_bit_cast(u32x4, wf[z % D]);
          acc[t] = __builtin_amdgcn_mfma_f32_32x32x8bf16_1k(__builtin_bit_cast(s16x4, (u32x2){e[0], e[1]}), __builtin_bit_cast(s16x4, mf), acc[t], 0, 0, 0);
        } else {
          constexpr int wu = f >> 4, i = f & 15;
          constexpr bool beta = wu >= 10 && wu < 15;
          constexpr int t = beta ? (i & 1) : (i & 3);
          constexpr int xi = wu < 10 ? 4 * wu + (i >> 2) : (beta ? 8 * (wu - 10) + (i >> 1) : 4 * (wu - 15) + (i >> 2));
          if constexpr (xi == 0) acc[t] = mfma32(wf[z % D], xf[xi], (f32x16){});
          else acc[t] = mfma32(wf[z % D], xf[xi], acc[t]);
        }
        SBAR();
        if constexpr (FINE == 4 && f >= 0 && (f & 15) == 8) STAMP(8 + (f >> 4));
        if constexpr (z == 10) { stat_load(); SBAR(); }
        if constexpr (z == 163) { STAMP(44); epi(IC<0>{}); STAMP(2); SBAR(); }
        if constexpr (z >= 166 && z < 166 + 8 * 9 && (z - 166) % 9 == 0) { if constexpr (!(ABL & 2)) trickle(IC<(z - 166) / 9>{}); SBAR(); }
        if constexpr (z == 247) { STAMP(45); epi(IC<1>{}); STAMP(3); SBAR(); }
        if constexpr (z == 247 + ZF1) {
          STAMP(47);
          wait_vm<0>();
          __builtin_amdgcn_s_setprio(3); put_flag(srd_ws, FL_1 + (tb * 32 + 2 * c + (w & 1)) * 8, tag); __builtin_amdgcn_s_setprio(0);
          STAMP(48);
          SBAR();
        }
        if constexpr (z >= 318 && z < 382 && (z & 3) == 2) { mask_setup(IC<(z - 318) / 4>{}); SBAR(); }
        if constexpr (z >= 382 && z < 401 && z % 3 == 1) { att_setup(IC<(z - 382) / 3>{}); SBAR(); }
        if constexpr (z == 411) {
          STAMP(46); epi(IC<2>{}); STAMP(4);
          *(LAS unsigned*)(sy + SY_DONE) = 25u;
          fw = 0u;
          await<1, SY_EV>(fw, sy);
          SBAR();
        }
      };
      static_for<0, 206>(slot);
      static_for<206, 412>(slot);
    }
    STAMP(5);
    {
      u32x4 q0[2], q1[2];
      f32x16 o[2];
      {
        f32x16 S[2];
        bf16x8 kf[2], kg[2];
        s16x4 vf[4];
        u32x4 pa[2], pb[2];
        q0[0] = *(const LAS u32x4*)aq; kg[0] = lds_b128(ak0);
        q1[0] = *(const LAS u32x4*)(aq + 8192); kg[1] = lds_b128(ak1);
        q0[1] = *(const LAS u32x4*)(aq + 512); kf[0] = lds_b128(ak0 + 512);
        q1[1] = *(const LAS u32x4*)(aq + 8192 + 512); kf[1] = lds_b128(ak1 + 512);
        SBAR();
        S[0] = mfma32(kg[0], asbf(q0[0]), mlo);
        S[0] = mfma32(kg[1], asbf(q1[0]), S[0]);
        SBAR();
        auto grp = [&](auto K_, auto G_) {
          constexpr int k = K_, gq = G_;
          float e[4];
          static_for<0, 4>([&](auto J_) {
            constexpr int j = J_;
            e[j] = __builtin_amdgcn_exp2f(S[k & 1][4 * gq + j]);
            SBAR();
            if constexpr (gq == 1 && j < 2 && (k & 1) && k < 16) kf[j] = lds_b128((j ? ak1 : ak0) + ((k + 3) >> 1) * 512);
            if constexpr (gq == 3 && ((k & 1) || k == 0)) vf[j] = lds_tr(av + ((k + 1) >> 1) * 512 + j * 128);
            SBAR();
          });
          u32x4& d = gq < 2 ? pa[k & 1] : pb[k & 1];
          d[2 * (gq & 1)] = pk2(e[0], e[1]); d[2 * (gq & 1) + 1] = pk2(e[2], e[3]);
          PIN(d);
          SBAR();
        };
        static_for<0, 19>([&](auto K_) {
          constexpr int k = K_, n = k + 1, pv = k - 1;
          if constexpr (k == KF2) { wait_vm<0>(); put_flag(srd_ws, FL_2 + (tb * 32 + 2 * c + (w & 1)) * 8, tag); SBAR(); }
          if constexpr (FINE == 3 && (k & 1) == 0 && k < 18) STAMP(8 + k);
          f32x16 sn;
          if constexpr (n < 18) { sn = mfma32(kf[0], asbf(q0[n & 1]), n == 1 ? mlo : (n >= 16 ? mhi : (f32x16){})); PIN(sn); SBAR(); }
          if constexpr (k < 18) grp(K_, IC<0>{});
          if constexpr (n < 18) { sn = mfma32(kf[1], asbf(q1[n & 1]), sn); PIN(sn); SBAR(); }
          if constexpr (k < 18) grp(K_, IC<1>{});
          if constexpr (pv >= 0) { o[pv & 1] = mfma32(join(vf[0], vf[1]), asbf(pa[pv & 1]), pv < 2 ? (f32x16){} : o[pv & 1]); PIN(o[pv & 1]); SBAR(); }
          if constexpr (k < 18) grp(K_, IC<2>{});
          if constexpr (pv >= 0) { o[pv & 1] = mfma32(join(vf[2], vf[3]), asbf(pb[pv & 1]), o[pv & 1]); PIN(o[pv & 1]); SBAR(); }
          if constexpr (k < 18) grp(K_, IC<3>{});
          if constexpr (n < 18) S[n & 1] = sn;
        });
      }
      if constexpr (FINE == 3) STAMP(26);
      const int lane = fresh_lane(), l31 = lane & 31, hh = lane >> 5;
      if (!__all(o[0][12] < 1e18f && o[1][12] < 1e18f && o[0][12] > 1e-18f && o[1][12] > 1e-18f)) {
#pragma unroll
        for (int a = 0; a < 2; ++a) {
          q1[a][0] = hh ? q1[a][0] & 0xffffu : q1[a][0];
          float mx = -__builtin_inff();
          o[a] = (f32x16){};
#pragma unroll 1
          for (int it = 0; it < 18; ++it) {
            const int i = it < 9 ? it : it - 9;
            f32x16 t = mfma32(lds_b128(ak0 + (a + i) * 512), asbf(q0[a]), (f32x16){});
            t = mfma32(lds_b128(ak1 + (a + i) * 512), asbf(q1[a]), t);
#pragma unroll
            for (int r = 0; r < 16; ++r) {
              const unsigned dist = 256 + l31 - 32 * i - ((r & 3) + 8 * (r >> 2) + 4 * hh);
              t[r] = dist < 256u ? t[r] : -__builtin_inff();
            }
            if (it < 9) {
#pragma unroll
              for (int r = 0; r < 16; r += 2) mx = max3(mx, t[r], t[r + 1]);
              if (it == 8) mx = swap_max32(mx);
            } else {
              u32x4 p0, p1;
#pragma unroll
              for (int r = 0; r < 4; ++r) {
                p0[r] = pk2(__builtin_amdgcn_exp2f(t[2 * r] - mx), __builtin_amdgcn_exp2f(t[2 * r + 1] - mx));
                p1[r] = pk2(__builtin_amdgcn_exp2f(t[8 + 2 * r] - mx), __builtin_amdgcn_exp2f(t[9 + 2 * r] - mx));
              }
              const unsigned vb = av + (a + i) * 512;
              o[a] = mfma32(join(lds_tr(vb), lds_tr(vb + 128)), asbf(p0), o[a]);
              o[a] = mfma32(join(lds_tr(vb + 256), lds_tr(vb + 384)), asbf(p1), o[a]);
            }
          }
        }
      }
      __builtin_amdgcn_s_setprio(3);
      const unsigned so = WS_A2 + (4 * qb + w) * A2_MT;
#pragma unroll
      for (int a = 0; a < 2; ++a) {
        const float il = __builtin_amdgcn_rcpf(o[a][12]);
        st16c<0>((u32x4){pk2(o[a][0] * il, o[a][1] * il), pk2(o[a][2] * il, o[a][3] * il), pk2(o[a][4] * il, o[a][5] * il), pk2(o[a][6] * il, o[a][7] * il)},
                 hh * 1024 + l31 * 16, srd_ws, so + (192 + 2 * head) * 1024 + a * 512);
        st8c<0>((u32x2){pk2(o[a][8] * il, o[a][9] * il), pk2(o[a][10] * il, o[a][11] * il)}, hh ? OOB : l31 * 8, srd_ws,
                so + 256 * 1024 + head * 512 + a * 256);
      }
    }
    STAMP(6);
    {
      const int lane = fresh_lane();
      const unsigned d_a = L0 + w * 4096, so_a = WS_A2 + mt * A2_MT + w * 4096, l16 = lane * 16;
      auto adma = [&](auto R_) { constexpr int r = R_; dma16c_x4<a_slot(r)>(d_a, l16, srd_ws, so_a + rnd(r) * 16384); };
      *(LAS unsigned*)(sy + SY_DONE) = 26u;
      wait_vm<0>();
      put_flag(srd_ws, FL_3 + ((4 * qb + w) * 32 + head) * 8, tag);
      STAMP(35);
      const int id = 64 * w + lane;
      ld16<0>(xres[0], (id / 10) * 1280 + (id % 10) * 16, srd_x, so_out);
      ld16<0>(xres[1], w < 2 ? ((id + 512) / 10) * 1280 + ((id + 512) % 10) * 16 : OOB, srd_x, so_out);
      unsigned dn = 0u;
      await<25, SY_DONE>(dn, L0 + L_SY + (lane & 3) * 64);
      __builtin_amdgcn_s_sleep(O_SLEEP);
      STAMP(36);
      poll(srd_ws, l16 & 255, jn * FL_REP + FL_3 + mt * 256, tag);
      PIN(xres[0]); PIN(xres[1]);
      STAMP(37);
      static_for<12, 14>(adma);
      static_for<0, 5>([&](auto J_) {
        if constexpr (J_ < 3) adma(IC<14 + J_>{});
        wait_vm<(J_ < 3 ? 8 : 16 - 4 * J_)>();
        *(LAS unsigned*)(sy + SY_DONE) = 27u + J_;
        SBAR();
      });
      STAMP(39);
      wait_bar<0>();
      STAMP(7);
    }
  } else {
    ROLE_SETUP();
    __builtin_amdgcn_s_setprio(2);
    const int s = w - 4;
    const int lane = fresh_lane();
    const int rho = lane >> 3;
    unsigned vo_x = (32 * (rho >> 1) + (rho & 1)) * 1280 + (lane & 7) * 16;
    unsigned vo_q = ((rho & 1) + 4 * (rho >> 1)) * 3840 + (lane & 7) * 16;
    unsigned vo_g = ((rho & 1) + 4 * (rho >> 1)) * 3072 + (lane & 7) * 16;
    unsigned st_al = 3840 - 768 * tyB;
    unsigned vo_al[4];
#pragma unroll
    for (int m = 0; m < 4; ++m) { vo_al[m] = (tyB ? vo_g : vo_q) + m * 16 * st_al; PIN(vo_al[m]); }
    unsigned c_x = (p * 128 + 8 * s) * 1280, c_al = (ci + (1 - tyB) * (2 * ci + (s >> 1))) * 128 + 2 * (s & 1) * st_al;
    unsigned d_x = L0 + s * 4160, d_2 = L0 + s * 4128;
    int rot = ((10 * (c & 3) + 5 * (p & 7)) >> 2) + RSH * (p >> 3);
    PINS(rot);
    rot = rot >= 10 ? rot - 10 : rot;
    PIN(vo_x); PINS(c_x); PINS(c_al); PINS(d_x); PINS(d_2); PINS(rot); PINS(st_al);
    SBAR();
    ARGS_READY();
    const u32x4 srd_x = SRD(ka, 0), srd_ws = SRD(kb, 0);
    const u32x4 srd_gu = (s >> 1) ? SRD(ka, 3) : SRD(ka, 2);
    const u32x4 srd_al = tyB ? srd_gu : SRD(ka, 1);
    srd_o = SRD(kb, 3);
    u32x4 srd_be, srd_rp;
    unsigned c_be, c_ga, d_1, vo_rp, so_rp[4];
    auto kst = [&](int n) INL { int k = n + rot; PINS(k); return k >= 10 ? k - 10 : k; };
    unsigned so;
    auto piece = [&](auto Q_) {
      constexpr int k = Q_ >> 2, m = Q_ & 3;
      constexpr unsigned d = upos(k);
      constexpr int wi = k == U_RP ? 0 : windex(k);
      if constexpr (k < 20 && !(k & 1)) {
        if constexpr (m == 0) so = c_x + kst(k >> 1) * 128;
        dma16<d + m * 1040, m * 2560>(d_x, vo_x, srd_x, so);
      } else if constexpr (k < 20) {
        if constexpr (m == 0) so = c_al + kst(k >> 1) * 64 * st_al;
        dma16<d + m * 1024, 0>(d_2, vo_al[m], srd_al, so);
      } else if constexpr (k == U_RP) {
        dma16<d + m * 1024, 0>(d_2, vo_rp, srd_rp, so_rp[m]);
      } else if constexpr (wi < 15) {
        if constexpr (m == 0) so = c_be + kst(2 * (wi - 10) + (s >> 1)) * (64 * 3840);
        dma16<d + m * 1024, m * 16 * 3840>(d_1, vo_q, srd_be, so);
      } else {
        if constexpr (m == 0) so = c_ga + kst(wi - 15) * (64 * 3072);
        dma16<d + m * 1024, m * 16 * 3072>(d_2, vo_g, srd_gu, so);
      }
    };
    STAMP(60);
    u32x2 ep;
    static_for<0, 4>(piece);
    ld8c<FL_EPOCH>(ep, 0u, srd_ws, 0u);
    static_for<4, 16>(piece);
    SBAR();

    so_out = mt * (64 * 1280) + jn * 160;
    srd_be = SRD(ka, 1); srd_be[2] = ub < 30 ? srd_be[2] : 0u;
    c_be = (ub < 30 ? ub : 0) * 128 + 2 * (s & 1) * 3840; c_ga = pg * 128 + 2 * (s & 1) * 3072;
    d_1 = L0 + (s & 1) * 8256 + (s >> 1) * 4096;
    const int ract = (s < 3) & (b * 3 + s < 680);
    const int ru = ract * (b * 3 + s), rs = ru / 10, rbl = ru - 10 * rs;
    {
      const int dn = rs < 48, tl = rs >= 64;
      srd_rp = dn ? SRD(kb, 1) : SRD(kb, 2);
      vo_rp = ract ? (tl ? 20 * (rho >> 2) + (rho & 3) : rho) * 1280 + (lane & 7) * 16 : OOB;
      const int r0 = rs * (40 - 8 * dn + 120 * tl) - 1920 * (1 - dn) - 8304 * tl;
      const int d1 = 8 + 32 * tl, d2 = 20 - 4 * dn + 60 * tl;
#pragma unroll
      for (int m = 0; m < 4; ++m) { so_rp[m] = (r0 + (m & 1) * d1 + (m >> 1) * d2) * 1280 + rbl * 128; PINS(so_rp[m]); }
      PIN(vo_rp); PINS(srd_rp);
    }
    const unsigned l16 = lane * 16, d_s = L0 + s * 1024;
    const int l31 = lane & 31, hh = lane >> 5;
    u64 tag = 0;
    const unsigned selw = ((lane & 15) >> 2) == (lane >> 4) ? 0x3f803f80u : 0u;
    const bf16x8 sel = asbf((u32x4){selw, selw, selw, selw});
    const unsigned lcs = ((lane & 15) >> 2) * 128 + (lane >> 4) * 32 + 8 * (lane & 3);
    const int gap = ((10 * (c & 3) + 5 * (p & 7) + 5) >> 2) - ((10 * (c & 3) + 5 * (p & 7)) >> 2) == 2;
    const unsigned so_cs = WS_CS + ((p >> 3) * 16 + c) * CS_SET + s * 5120;
    bf16x8 cf[4];
    auto cs_read = [&](auto K_) {
      constexpr int k = K_;
      unsigned ac = ((k == 20 || k == 21 || (k >= 24 && k < 27)) ? d_1 : d_2) + upos(k) + lcs; PIN(ac);
#pragma unroll
      for (int i = 0; i < 4; ++i) cf[i] = join(lds_tr(ac + i * 1024), lds_tr(ac + i * 1024 + 512));
    };
    auto cs_add = [&](f32x4& cs) INL {
#pragma unroll
      for (int i = 0; i < ((ABL & 1) ? 0 : 4); ++i) cs = mfma16(sel, cf[i], cs);
      PIN(cs);
      SBAR();
    };
    auto put_e = [&](bool beta, int col, float v) INL {
      const int gq = (col >> 2) & 3, n = 4 * (((gq & 1) << 1) | (gq >> 1)) + (col & 3);
      const int e = beta ? ((col >> 5) + 2 * (s >> 1)) * 64 + (col & 16) + n : (col >> 4) * 64 + 16 * (s >> 1) + n;
      *(LAS u32x2*)(L0 + L_E + (e + 32 * (s & 1)) * 8) = split3(v);
    };
    const unsigned sy = L0 + L_SY + (lane & 3) * 64;
    unsigned dn = 0u;
    unsigned syw = sy + 4 * s;
    PIN(syw);
    auto post = [&](auto OFF_, unsigned v) INL {
      asm volatile("s_waitcnt lgkmcnt(0)" ::: "memory");
      *(LAS unsigned*)(syw + OFF_) = v;
      asm volatile("" ::: "memory");
    };
    auto full = [&](unsigned v) INL { post(IC<SY_FULL>{}, v); };
    auto event = [&](unsigned v) INL { post(IC<SY_EV>{}, v); };
    u32x4 stg;
    f32x4 csa = {}, csb = {};
    static_for<0, 9>([&](auto N_) {
      constexpr int n = N_;
      constexpr int e0 = n == 0 ? 4 : (n == 1 ? 7 : 2 * n + 6), e1 = n == 0 ? 7 : 2 * n + 8;
      if constexpr (FINE == 1) STAMP(8 + n);
      wait_all<(n < 8 ? 4 * (e0 - 2 * n - 2) : 9)>();
      cs_read(IC<2 * n + 1>{});
      bar();
      if constexpr (FINE == 1) STAMP(35 + n);
      if constexpr (n == 7) ld16c<0>(stg, l31 * 16, srd_ws, WS_STAT + (p * 128 + 32 * s) * 16);
      piece(IC<4 * e0>{});
      cs_add(csa);
      if constexpr (n == 8) { cs_read(IC<19>{}); piece(IC<4 * e0 + 1>{}); cs_add(csa); }
      static_for<4 * e0 + 1 + (n == 8), 4 * e1>(piece);
      SBAR();
    });
    PIN(ep); tag = uni64(ep) + 1;
    put_e(false, lane, csa[0]);
    wait_vm<16>();
    PIN(stg);
    {
      const unsigned tl = (unsigned)tag << 16, th = (unsigned)(tag >> 16);
#pragma unroll 1
      for (unsigned spin = 0; spin < SPIN_CAP; ++spin) {
        if (__all(((stg[0] ^ tl) | (stg[2] ^ tl)) < 65536u && stg[1] == th && stg[3] == th)) break;
        __builtin_amdgcn_s_sleep(POLL_SLEEP);
        ld16c<0>(stg, l31 * 16, srd_ws, WS_STAT + (p * 128 + 32 * s) * 16);
        wait_vm<0>();
        PIN(stg);
      }
      *(LAS u32x2*)(L0 + L_ST + (32 * s + l31) * 8) = (u32x2){(stg[0] & 0xffffu) | (stg[0] << 16), __float_as_uint(__builtin_amdgcn_rcpf(__uint_as_float(stg[2] << 16)))};
    }
    if constexpr (FINE == 1) STAMP(18);
    wait_all<8>();
    cs_read(IC<20>{});
    bar();
    if constexpr (FINE == 1) STAMP(45);
    static_for<4 * 24, 4 * 25>(piece);
    cs_add(csb);
    cs_read(IC<21>{});
    static_for<4 * 25, 4 * 26>(piece);
    cs_add(csb);
    static_for<4 * 26, 4 * 28>(piece);
    wait_vm<20>();
    {
      const unsigned ar = d_2 + upos(U_RP) + (8 * (lane >> 4) + ((lane & 15) >> 2)) * 128 + 8 * (lane & 3);
      const unsigned vs = ract ? l16 : OOB;
#pragma unroll
      for (int t = 0; t < 4; ++t) {
        const int ct = 4 * rbl + t, cj = (ct * 13) >> 6;
        const bf16x8 f = join(lds_tr(ar + 32 * t), lds_tr(ar + 32 * t + 512));
        st16c<0>(__builtin_bit_cast(u32x4, f), vs, srd_ws, WS_R + ((cj * 68 + rs) * 5 + ct - 5 * cj) * 1024);
      }
    }
    if constexpr (FINE == 1) STAMP(20);
    wait_all<8>();
    cs_read(IC<24>{});
    bar();
    if constexpr (FINE == 1) STAMP(47);
    static_for<4 * 28, 4 * 28 + 2>(piece);
    cs_add(csb);
    cs_read(IC<25>{});
    static_for<4 * 28 + 2, 4 * 29 + 1>(piece);
    cs_add(csb);
    cs_read(IC<26>{});
    static_for<4 * 29 + 1, 4 * 30>(piece);
    cs_add(csb);
    put_e(true, lane, csb[0]);
    if constexpr (FINE == 1) STAMP(23);
    wait_bar<0>();
    if constexpr (FINE == 1) STAMP(50);
    put_flag(srd_ws, FL_0 + b * 8, tag, s == 0 ? 8 : 0);
    static_for<0, 2>([&](auto J_) {
      f32x4 cg = {};
      cs_read(IC<(J_ ? 27 : 23)>{}); cs_add(cg);
      const u32x2 gr = {((__float_as_uint(cg[0]) + 0x80u) & 0xffffff00u) | ((unsigned)tag & 0xffu), (unsigned)(tag >> 8)};
      st8c<0>(gr, (!J_ || gap) ? lane * 8 : OOB, srd_ws, so_cs + kst(J_) * 512);
    });
    __builtin_amdgcn_s_sleep(EPI_NAP);
    static_for<4 * 30, 4 * 34>(piece);
    if constexpr (FINE == 1) STAMP(25);

    u32x4 br[85], ar[16];
    const unsigned so_b = WS_R + jn * R_J + s * 5120;
    unsigned sob, vb_early;
    auto bload = [&](auto I_) {
      constexpr int i = I_;
      if constexpr (i % 5 == 0) sob = so_b + rnd(i / 5) * 20480;
      if constexpr (i % 5 == 4) { sob += 4096; PINS(sob); }
      ld16c<(i % 5 < 4 ? (i % 5) * 1024 : 0)>(br[i], i < NB0 ? vb_early : l16, srd_ws, sob);
    };
    const unsigned d_a = L0 + s * 4096, so_a = WS_A2 + mt * A2_MT + s * 4096;
    unsigned aa = (lane >> 4) * 1024 + (lane & 15) * 16;
    PIN(aa);
    unsigned soa;
    auto aload = [&](auto I_) {
      constexpr int i = I_;
      if constexpr ((i & 3) == 0) soa = so_a + rnd(i >> 2) * 16384;
      ld16c<(i & 3) * 256>(ar[i], aa, srd_ws, soa);
    };
    {
      u32x4 f0a, f0b;
      u32x4 cg[5], f1;
      auto cs_load = [&]() INL {
        ld16c<0>(cg[0], l16, srd_ws, so_cs); ld16c<1024>(cg[1], l16, srd_ws, so_cs); ld16c<2048>(cg[2], l16, srd_ws, so_cs); ld16c<3072>(cg[3], l16, srd_ws, so_cs);
        ld16c<0>(cg[4], l16, srd_ws, so_cs + 4096);
      };
      await<15, SY_DONE, NAPS>(dn, sy);
      static_for<4 * 34, 4 * 36>(piece);
      ld16c<FL_0>(f0a, l16, srd_ws, xcd * FL_REP); ld16c<FL_0 + 1024>(f0b, l16, srd_ws, xcd * FL_REP);
      cs_load();
      wait_vm<15>(); full(1);
      wait_vm<5>(); full(2);
      PIN(f0a); PIN(f0b);
      const bool early = __all(tagged(f0a, tag) && tagged(f0b, tag));
      vb_early = early ? l16 : OOB;
      STAMP(26);
      const int tq = 4 * qb + s, sel3 = lane >> 4;
      const unsigned vf1 = (sel3 == 0 ? (qb ? tq - 4 : tq) : (sel3 == 1 ? tq : mt)) * 256 + (l16 & 255);
      ld16c<0>(f1, vf1, srd_ws, xcd * FL_REP + FL_1);
      static_for<0, NB0>(bload);
      wait_vm<NB0>();
      PIN(cg[0]); PIN(cg[1]); PIN(cg[2]); PIN(cg[3]); PIN(cg[4]); PIN(f1);
      STAMP(27);
      const unsigned s1o = WS_QKV + (4 * qb + s) * QKV_TB + o0 * 1024, s0o = qb ? s1o - 4 * QKV_TB : s1o;
      auto qk_fill = [&]() INL {
        await<17, SY_DONE, NAPS>(dn, sy);
        static_for<0, 3>([&](auto J_) {
          constexpr int i = J_;
          dma16c<L_Q + i * 4096, i * 1024>(d_s, l16, srd_ws, s1o);
          dma16c<krow(i) + 4096, (80 + i) * 1024>(d_s, l16, srd_ws, s1o);
        });
      };
      __builtin_amdgcn_s_setprio(3);
      const bool ready = __all(tagged(f1, tag));
      if (ready) qk_fill();
#pragma unroll 1
      for (unsigned spin = 0; spin < SPIN_CAP; ++spin) {
        unsigned bad = 0;
#pragma unroll
        for (int j = 0; j < 5; ++j)
          bad |= (((cg[j][0] ^ (unsigned)tag) | (cg[j][2] ^ (unsigned)tag)) & 0xffu) | (cg[j][1] ^ (unsigned)(tag >> 8)) | (cg[j][3] ^ (unsigned)(tag >> 8));
        if (__all(bad == 0u)) break;
        __builtin_amdgcn_s_sleep(POLL_SLEEP);
        cs_load();
        wait_vm<0>();
        PIN(cg[0]); PIN(cg[1]); PIN(cg[2]); PIN(cg[3]); PIN(cg[4]);
      }
      {
        float v0 = 0.f, v1 = 0.f;
#pragma unroll
        for (int j = 0; j < 5; ++j) { v0 += __uint_as_float(cg[j][0] & 0xffffff00u); v1 += __uint_as_float(cg[j][2] & 0xffffff00u); }
        put_e(false, 2 * l31 + hh, swap_add32(v0, v1));
        event(1);
      }
      if (!ready) { poll(srd_ws, vf1, xcd * FL_REP + FL_1, tag); qk_fill(); }
      STAMP(1);
      await<19, SY_DONE, NAPS>(dn, sy);
      __builtin_amdgcn_s_setprio(2);
#pragma unroll
      for (int hb = 0; hb < 2; ++hb)
        *(LAS u32x4*)(L0 + L_FR + (256 * hb + 64 * s) * 16 + l16) = (u32x4){(qb == 0 && hb == 0) ? 0x3f803f80u : 0x3f800000u, 0x3f803f80u, 0x3f803f80u, 0x3f803f80u};
      static_for<0, 3>([&](auto J_) { constexpr int i = J_; dma16c<(3 + i) * AROW + 4096, (160 + i) * 1024>(d_s, l16, srd_ws, s1o); });
      static_for<0, 3>([&](auto J_) {
        constexpr int i = J_;
        dma16c<krow(i), (80 + i) * 1024>(d_s, l16, srd_ws, s0o);
        dma16c<(3 + i) * AROW, (160 + i) * 1024>(d_s, l16, srd_ws, s0o);
      });
      __builtin_amdgcn_s_setprio(1);
      static_for<0, 6>(aload);
      __builtin_amdgcn_s_setprio(3);
      wait_vm<15>();
      STAMP(53);
      const unsigned aq = d_s + L_Q + l31 * 16, ak = d_s + 4096 + l31 * 16;
      u32x4 q0[2], q1[2];
      f32x16 t[2];
#pragma unroll
      for (int a = 0; a < 2; ++a) {
        q0[a] = *(const LAS u32x4*)(aq + hh * 4096 + a * 512); q1[a] = *(const LAS u32x4*)(aq + 8192 + a * 512);
        const bool z0 = par && !hh;
        q0[a][0] = z0 ? 0u : q0[a][0]; q0[a][1] = z0 ? 0u : q0[a][1];
        q1[a][0] = hh ? 0u : q1[a][0]; q1[a][1] = hh ? 0u : q1[a][1];
        q1[a][2] = z0 ? q1[a][2] : 0u; q1[a][3] = z0 ? q1[a][3] : 0u;
        t[a] = mfma32(lds_b128(ak + hh * AROW + a * 512), asbf(q0[a]), (f32x16){});
        t[a] = mfma32(lds_b128(ak + L_K2 + a * 512), asbf(q1[a]), t[a]);
        *(LAS u32x2*)(aq + hh * 4096 + a * 512) = (u32x2){q0[a][0], q0[a][1]};
      }
#pragma unroll
      for (int a = 0; a < 2; ++a) {
        float mx = __builtin_amdgcn_fmed3f(t[a][0], t[a][1], __builtin_inff()), my = max3(mx, t[a][2], t[a][3]);
#pragma unroll
        for (int r = 4; r < 16; r += 4) { mx = max3(mx, t[a][r], t[a][r + 1]); my = max3(my, t[a][r + 2], t[a][r + 3]); }
        const unsigned ref = pk2(-1e30f, -swap_max32(max3(mx, my, my)));
        q1[a][0] = hh ? ref : q1[a][0];
        *(LAS u32x4*)(aq + 8192 + hh * 4096 + a * 512) = q1[a];
      }
      SBAR();
      STAMP(54);
      wait_vm<6>();
      event(2);
      __builtin_amdgcn_s_setprio(0);
      STAMP(2);
      _Pragma("unroll") for (int i = 0; i < NB0; ++i) PIN(br[i]);
      if (!early) {
        poll(srd_ws, l16, xcd * FL_REP + FL_0, tag); poll(srd_ws, l16, xcd * FL_REP + FL_0 + 1024, tag);
        vb_early = l16;
        static_for<0, NB0>(bload);
        wait_vm<0>();
        _Pragma("unroll") for (int i = 0; i < NB0; ++i) PIN(br[i]);
      }
      put_flag(srd_ws, FL_EPOCH, tag, b == 0 && s == 0 ? 1 : 0);
      static_for<6, 8>(aload);
    }
    STAMP(28);
    await<24, SY_DONE, NAPS>(dn, sy);
    STAMP(3);
    {
      auto adma = [&](auto R_) { constexpr int r = R_; dma16c_x4<a_slot(r)>(d_a, l16, srd_ws, so_a + rnd(r) * 16384); };
      STAMP(4);
      bf16x8 af[4];
      f32x4 acc[4][5];
      unsigned cur, ap;
      u32x4 f2;
      constexpr auto bend = [](int q) constexpr { int e = NB0; for (int i = 0; i <= q; ++i) e = imin(imin(e + (i < 2 ? 6 : 10), BCAP + 5 * i), 85); return e; };
      auto aread = [&](auto R_, auto I_) {
        constexpr int r = R_, i = I_;
        if constexpr (r < 16) af[i] = lds_b128(cur + i * 256);
        else {
          const u32x2 lo = *(const LAS u32x2*)(cur + i * 128), hi = *(const LAS u32x2*)(cur + i * 128 + 512);
          af[i] = asbf((u32x4){lo[0], lo[1], hi[0], hi[1]});
        }
        SBAR();
      };
      auto step = [&](auto R_, auto Q_) {
        constexpr int r = R_, q = Q_;
        if constexpr (r >= 0 && r < 16) { PIN(aa); cur = aa + d_a + a_slot(r); PIN(cur); }
        if constexpr (r == 16) { const int ln = fresh_lane(); cur = d_a + a_slot(r) + (ln >> 4) * 1024 + (ln & 15) * 8; PIN(cur); }
        SBAR();
        static_for<0, 20>([&](auto M_) {
          constexpr int m = M_, i = m / 5, t = m % 5;
          if constexpr (q >= 0) {
            if constexpr (i == 0) PIN(br[5 * q + t]);
            if constexpr (q < 4 && t == 0) PIN(ar[4 * q + i]);
            acc[i][t] = mfma16(asbf(br[5 * q + t]), q < 4 ? asbf(ar[(4 * q + i) & 15]) : af[i], q == 0 ? (f32x4){} : acc[i][t]);
            SBAR();
            constexpr int nb = bend(q) - bend(q - 1), j = nb > 5 ? ((m & 1) ? m >> 1 : 99) : ((m & 3) == 1 ? m >> 2 : 99);
            if constexpr (j < nb) { bload(IC<bend(q - 1) + j>{}); SBAR(); }
            else if constexpr (q < 2 && j < 10) { aload(IC<4 * q + 8 + j - 6>{}); SBAR(); }
            if constexpr (q == 16 && i > 0) { *(LAS f32x4*)(ap + 16 * (i - 1) * PROW + 64 * t) = acc[i - 1][t]; SBAR(); }
          }
          if constexpr (r >= 0 && t == 4) aread(R_, IC<i>{});
        });
        if constexpr (q >= 4 && q < 8) { adma(IC<q + 4>{}); SBAR(); }
      };
      wait_vm<2>();
      STAMP(5);
      step(IC<-1>{}, IC<0>{}); wait_vm<10>();
      step(IC<-1>{}, IC<1>{}); wait_vm<10>();
      step(IC<-1>{}, IC<2>{}); wait_vm<10>();
      ld16c<0>(f2, l16 & 255, srd_ws, jn * FL_REP + FL_2 + mt * 256);
      step(IC<-1>{}, IC<3>{});
      STAMP(6);
      wait_vm<bend(3) - bend(2)>();
      PIN(f2);
      if (!__all(tagged(f2, tag))) poll(srd_ws, l16 & 255, jn * FL_REP + FL_2 + mt * 256, tag);
      STAMP(55);
      static_for<4, 8>(adma);
      STAMP(56);
      wait_vm<12>();
      step(IC<4>{}, IC<-1>{});
      constexpr auto nops = [bend](int q0, int q1) constexpr { int n = 0; for (int q = q0; q < q1; ++q) n += bend(q) - bend(q - 1) + (q < 8 ? 4 : 0); return n; };
      constexpr auto young_b = [bend, nops](int i, int q) constexpr {
        if (i < bend(3)) return 63;
        int u = 4; while (bend(u) <= i) ++u;
        return bend(u) - 1 - i + (u < 8 ? 4 : 0) + nops(u + 1, q);
      };
      unsigned oc = 0u;
      const unsigned syo = L0 + L_SY + s * 64;
      static_for<5, 18>([&](auto R_) {
        constexpr int r = R_, q = r - 1;
        if constexpr (FINE == 2) STAMP(8 + r);
        constexpr int na = r < 8 ? 4 * (7 - r) + nops(4, q) : (r < 12 ? nops(r - 3, q) : 63);
        wait_vm<imin(na, young_b(5 * q + 4, q))>();
        if constexpr (r >= 12 && r < 17) { unsigned a = syo; PIN(a); await<14 + r, SY_DONE>(oc, a); }
        if constexpr (r == 12) __builtin_amdgcn_s_setprio(2);
        if constexpr (r == 16) bar();
        if constexpr (FINE == 2) STAMP(35 + r);
        if constexpr (r == 14) {
          const int id = 64 * w + fresh_lane();
          ld16<0>(xres[0], (id / 10) * 1280 + (id % 10) * 16, srd_x, so_out);
        }
        if constexpr (r == 17) {
          wait_vm<0>();
          PIN(xres[0]); xres[1] = (u32x4){0u, 0u, 0u, 0u};
          const int ln = fresh_lane();
          ap = L0 + s * PCLS + (ln & 15) * PROW + 16 * (ln >> 4);
          PIN(ap);
        }
        step(IC<(r < 17 ? r : -1)>{}, IC<q>{});
      });
#pragma unroll
      for (int t = 0; t < 5; ++t) *(LAS f32x4*)(ap + 48 * PROW + 64 * t) = acc[3][t];
      STAMP(7);
    }
  }
  {
    const int lane = fresh_lane();
    unsigned ra[2], go[2];
#pragma unroll
    for (int it = 0; it < 2; ++it) {
      const int id = 64 * (it ? (w & 1) + 8 : w) + lane, row = id / 10, cc = id - 10 * row;
      ra[it] = L0 + row * PROW + cc * 32; go[it] = row * 1280 + cc * 16;
      PIN(ra[it]); PIN(go[it]);
    }
    wait_bar<0>();
    PIN(xres[0]); PIN(xres[1]);
    auto fin = [&](int it) INL {
      f32x4 lo = *(const LAS f32x4*)(ra[it] + 3 * PCLS), hi = *(const LAS f32x4*)(ra[it] + 3 * PCLS + 16);
#pragma unroll
      for (int k = 0; k < 3; ++k) { lo += *(const LAS f32x4*)(ra[it] + k * PCLS); hi += *(const LAS f32x4*)(ra[it] + k * PCLS + 16); }
      const u32x4 xv = xres[it];
      u32x4 ov;
      ov[0] = pk2(lo[0] + __uint_as_float(xv[0] << 16), lo[1] + __uint_as_float(xv[0] & 0xffff0000u));
      ov[1] = pk2(lo[2] + __uint_as_float(xv[1] << 16), lo[3] + __uint_as_float(xv[1] & 0xffff0000u));
      ov[2] = pk2(hi[0] + __uint_as_float(xv[2] << 16), hi[1] + __uint_as_float(xv[2] & 0xffff0000u));
      ov[3] = pk2(hi[2] + __uint_as_float(xv[3] << 16), hi[3] + __uint_as_float(xv[3] & 0xffff0000u));
      return ov;
    };
    if (w < 2) {
      const u32x4 o0v = fin(0), o1v = fin(1);
      st16<0>(o0v, go[0], srd_o, so_out);
      st16<0>(o1v, go[1], srd_o, so_out);
    } else {
      st16<0>(fin(0), go[0], srd_o, so_out);
    }
  }
  STAMP(33);
  if (w == 0 && (blockIdx.x & 63) == 21) { const u64 until = pr_0 + 5000 + 5 * (pr_t - pr_0); while (__builtin_amdgcn_s_memrealtime() < until) __builtin_amdgcn_s_sleep(1); }
  asm volatile(".Lcode_end:");
#endif
}

extern "C" void kernel_launch(void* const* d_in, const int* in_sizes, int n_in, void* d_out, int out_size, void* d_ws, size_t ws_size,
                              hipStream_t stream) {
  const unsigned mode = 0x00020000u;
  Params P;
  P.x = {d_in[0], SEQ * DM * 2, mode};
  P.wqkv = {d_in[1], DM * 3 * DM * 2, mode};
  P.wo = {d_in[2], DM * DM * 2, mode};
  P.wg = {d_in[4], DM * FF * 2, mode};
  P.wu = {d_in[5], DM * FF * 2, mode};
  P.wd = {d_in[6], FF * DM * 2, mode};
  P.out = {d_out, SEQ * DM * 2, mode};
  P.ws = {d_ws, WS_STAMP, mode};
  fused<<<256, 512, 0, stream>>>(P);
}
```

```cpp
#include <hip/hip_runtime.h>
#include <stdint.h>
#include <type_traits>
#include <utility>

#define STAMPS 0
#define FINE 0
#define ABL 0
#define PRO 4
#define NB1 24
#define ZF1 28
#define KF2 3
#define CSV 2
#define POLL_SLEEP 6
#define NB0 15
#define BCAP 38
#define O_SLEEP 4
#define NAPS 2
#define MAP48 1
#define ROT 0
#define EPI_NAP 0
#define CLK "s_memtime"
#define RSH 2
#define PAIRS 0
#define DEPTH 5
#define NBX 8
#define WPRIO 0
#define NA0 8
#define NBH 45
#define NWX 4
#define NAE 4
#define NBF 6
#define QUP 6
#define RT 5
#define NLAUNCH 1

typedef __bf16 bf16x8 __attribute__((ext_vector_type(8)));
typedef __bf16 bf16x2 __attribute__((ext_vector_type(2)));
typedef short s16x4 __attribute__((ext_vector_type(4)));
typedef float f32x16 __attribute__((ext_vector_type(16)));
typedef float f32x4 __attribute__((ext_vector_type(4)));
typedef float f32x2 __attribute__((ext_vector_type(2)));
typedef unsigned u32x4 __attribute__((ext_vector_type(4)));
typedef unsigned u32x2 __attribute__((ext_vector_type(2)));
typedef unsigned u32x16 __attribute__((ext_vector_type(16)));
typedef unsigned long long u64;
#define LAS __attribute__((address_space(3)))
#define GAS __attribute__((address_space(1)))
#define DEV __device__ __forceinline__
#define INL __attribute__((always_inline))
template <int V> using IC = std::integral_constant<int, V>;

constexpr int SEQ = 2048, DM = 640, FF = 1536;
constexpr unsigned OOB = 0x7ff00000u;
constexpr unsigned QKV_TB = 240 * 1024;
constexpr unsigned A2_MT = 272 * 1024;
constexpr unsigned R_J = 68 * 5 * 1024;
constexpr unsigned FL_EPOCH = 0, FL_0 = 4096, FL_1 = 8192, FL_2 = 16384, FL_3 = 24576, FL_REP = 32768;
constexpr unsigned WS_STAT = 8 * FL_REP, WS_CS = WS_STAT + 32768, CS_SET = 4 * 5120, WS_QKV = 1048576, WS_A2 = WS_QKV + 32 * QKV_TB, WS_R = WS_A2 + 32 * A2_MT, WS_STAMP = WS_R + 8 * R_J;

constexpr unsigned UNIT = 16640;
static_assert(WS_CS + 32 * CS_SET <= WS_QKV, "workspace map");
constexpr unsigned AROW = 8256;
constexpr unsigned L_FR = 2 * AROW, L_K2 = 8 * UNIT, L_Q = L_K2 + 8192;
constexpr unsigned krow(int i) { return i < 2 ? i * AROW : L_K2; }
constexpr unsigned L_E = 160768, L_ST = L_E + 2048;
constexpr unsigned L_T = L_Q + 16384;
constexpr unsigned L_MK = L_K2 - 8192;
constexpr unsigned L_SY = L_E - 256, SY_FULL = 0, SY_EV = 16, SY_DONE = 32, SY_CNT = 48;
static_assert(AROW + 8192 <= UNIT && L_T + 1024 <= L_SY && L_T >= 9 * UNIT && L_MK + 8192 <= L_K2, "attention image");
constexpr unsigned L_W0 = 6 * AROW, L_P0 = 12288, L_P3 = L_W0 + 72 * 1024, L_W2 = L_P3 + 20480;
constexpr int NWR = 28;
constexpr unsigned w_slot(int j) { return j < 72 ? L_W0 + j * 1024 : (j < 88 ? L_W2 + (j - 72) * 1024 : (j - 88) * 1024); }
constexpr unsigned PROW = 336;
static_assert(L_P3 <= L_MK && w_slot(87) + 1024 <= L_SY && L_P0 + 3 * 20480 <= L_W0 + NWR * 1024, "phase-2 image");

template <int I, class F, int... J> DEV void static_for_impl(F& f, std::integer_sequence<int, J...>) { (f(IC<I + J>{}), ...); }
template <int I, int N, class F> DEV void static_for(F&& f) { if constexpr (I < N) static_for_impl<I>(f, std::make_integer_sequence<int, N - I>{}); }

template <unsigned LOFF, unsigned SOFF> DEV void dma16(unsigned lb, unsigned voff, u32x4 srd, unsigned sb) {
  unsigned t;
  asm volatile("s_add_u32 m0, %1, %5\n\ts_add_u32 %0, %4, %6\n\tbuffer_load_dwordx4 %2, %3, %0 offen lds"
               : "=&s"(t) : "s"(lb), "v"(voff), "s"(srd), "s"(sb), "i"(LOFF), "i"(SOFF) : "memory", "scc");
}
template <unsigned LOFF, unsigned SOFF> DEV void dma16c(unsigned lb, unsigned voff, u32x4 srd, unsigned sb) {
  unsigned t;
  asm volatile("s_add_u32 m0, %1, %5\n\ts_add_u32 %0, %4, %6\n\tbuffer_load_dwordx4 %2, %3, %0 offen sc1 lds"
               : "=&s"(t) : "s"(lb), "v"(voff), "s"(srd), "s"(sb), "i"(LOFF), "i"(SOFF) : "memory", "scc");
}
template <unsigned LOFF> DEV void dma16c_x4(unsigned lb, unsigned voff, u32x4 srd, unsigned sb) {
  asm volatile("s_add_u32 m0, %0, %4\n\ts_nop 0\n\tbuffer_load_dwordx4 %1, %2, %3 offen sc1 lds\n\t"
               "buffer_load_dwordx4 %1, %2, %3 offen offset:1024 sc1 lds\n\tbuffer_load_dwordx4 %1, %2, %3 offen offset:2048 sc1 lds\n\t"
               "buffer_load_dwordx4 %1, %2, %3 offen offset:3072 sc1 lds"
               :: "s"(lb), "v"(voff), "s"(srd), "s"(sb), "i"(LOFF) : "memory", "scc");
}
template <int OFF> DEV void ld16c(u32x4& v, unsigned voff, u32x4 srd, unsigned soff) {
  asm volatile("buffer_load_dwordx4 %0, %1, %2, %3 offen offset:%4 sc1" : "=v"(v) : "v"(voff), "s"(srd), "s"(soff), "i"(OFF) : "memory");
}
template <int OFF> DEV void reld16c(u32x4& v, unsigned voff, u32x4 srd, unsigned soff) {
  asm volatile("buffer_load_dwordx4 %0, %1, %2, %3 offen offset:%4 sc1" : "+v"(v) : "v"(voff), "s"(srd), "s"(soff), "i"(OFF) : "memory");
}
template <int OFF> DEV void ld8c(u32x2& v, unsigned voff, u32x4 srd, unsigned soff) {
  asm volatile("buffer_load_dwordx2 %0, %1, %2, %3 offen offset:%4 sc1" : "=v"(v) : "v"(voff), "s"(srd), "s"(soff), "i"(OFF) : "memory");
}
template <int OFF> DEV void ld16(u32x4& v, unsigned voff, u32x4 srd, unsigned soff) {
  asm volatile("buffer_load_dwordx4 %0, %1, %2, %3 offen offset:%4" : "=v"(v) : "v"(voff), "s"(srd), "s"(soff), "i"(OFF) : "memory");
}
template <int OFF> DEV void st16c(u32x4 v, unsigned voff, u32x4 srd, unsigned soff) {
  asm volatile("buffer_store_dwordx4 %0, %1, %2, %3 offen offset:%4 sc1\n\ts_nop 1" ::"v"(v), "v"(voff), "s"(srd), "s"(soff), "i"(OFF) : "memory");
}
template <int OFF> DEV void st16(u32x4 v, unsigned voff, u32x4 srd, unsigned soff) {
  asm volatile("buffer_store_dwordx4 %0, %1, %2, %3 offen offset:%4\n\ts_nop 1" ::"v"(v), "v"(voff), "s"(srd), "s"(soff), "i"(OFF) : "memory");
}
template <unsigned SOFF> DEV void st16p(u32x4 v, unsigned voff, u32x4 srd, unsigned sb) {
  unsigned t;
  asm volatile("s_setprio 3\n\ts_add_u32 %0, %4, %5\n\tbuffer_store_dwordx4 %1, %2, %3, %0 offen sc1\n\ts_setprio 0\n\ts_nop 0"
               : "=&s"(t) : "v"(v), "v"(voff), "s"(srd), "s"(sb), "i"(SOFF) : "memory", "scc");
}
template <int OFF> DEV void st8c(u32x2 v, unsigned voff, u32x4 srd, unsigned soff) {
  asm volatile("buffer_store_dwordx2 %0, %1, %2, %3 offen offset:%4 sc1" ::"v"(v), "v"(voff), "s"(srd), "s"(soff), "i"(OFF) : "memory");
}
template <int OFF> DEV void st4c(unsigned v, unsigned voff, u32x4 srd, unsigned soff) {
  asm volatile("buffer_store_dword %0, %1, %2, %3 offen offset:%4 sc1" ::"v"(v), "v"(voff), "s"(srd), "s"(soff), "i"(OFF) : "memory");
}
template <int N> DEV void wait_vm() { asm volatile("s_waitcnt vmcnt(%0)" ::"i"(N > 63 ? 63 : N) : "memory"); }
template <int N> DEV void wait_all() { asm volatile("s_waitcnt vmcnt(%0) lgkmcnt(0)" ::"i"(N > 63 ? 63 : N) : "memory"); }
template <int N> DEV void wait_bar() { asm volatile("s_waitcnt vmcnt(%0) lgkmcnt(0)\n\ts_barrier" ::"i"(N > 63 ? 63 : N) : "memory"); }
DEV void bar() { asm volatile("s_barrier" ::: "memory"); }
#define PIN(x) asm volatile("" : "+v"(x))
#define PINS(x) asm volatile("" : "+s"(x))
#define SBAR() __builtin_amdgcn_sched_barrier(0)
DEV int fresh_lane() { int l; asm volatile("v_mbcnt_lo_u32_b32 %0, -1, 0\n\tv_mbcnt_hi_u32_b32 %0, -1, %0" : "=v"(l)); return l; }

DEV bf16x8 lds_b128(unsigned a) { return *(const LAS bf16x8*)a; }
DEV s16x4 lds_tr(unsigned a) { return __builtin_amdgcn_ds_read_tr16_b64_v4i16((LAS s16x4*)a); }
DEV bf16x8 join(s16x4 lo, s16x4 hi) { return __builtin_bit_cast(bf16x8, __builtin_shufflevector(lo, hi, 0, 1, 2, 3, 4, 5, 6, 7)); }
DEV bf16x8 asbf(u32x4 v) { return __builtin_bit_cast(bf16x8, v); }
DEV f32x16 mfma32(bf16x8 a, bf16x8 b, f32x16 c) { return __builtin_amdgcn_mfma_f32_32x32x16_bf16(a, b, c, 0, 0, 0); }
DEV f32x4 mfma16(bf16x8 a, bf16x8 b, f32x4 c) { return __builtin_amdgcn_mfma_f32_16x16x32_bf16(a, b, c, 0, 0, 0); }
template <bool FIRST> DEV void mma16(f32x4& c, bf16x8 a, bf16x8 b) {
  if constexpr (FIRST) asm volatile("v_mfma_f32_16x16x32_bf16 %0, %1, %2, 0" : "=&v"(c) : "v"(a), "v"(b));
  else asm volatile("v_mfma_f32_16x16x32_bf16 %0, %1, %2, %0" : "+v"(c) : "v"(a), "v"(b));
}
DEV void mma16k16(f32x4& c, s16x4 a, u32x2 b) { asm volatile("v_mfma_f32_16x16x16_bf16 %0, %1, %2, %0" : "+v"(c) : "v"(a), "v"(b)); }
DEV void mma_settle() { asm volatile("s_nop 7\n\ts_nop 1"); }
DEV unsigned pk2(float lo, float hi) { return __builtin_bit_cast(unsigned, (bf16x2){(__bf16)lo, (__bf16)hi}); }
DEV float rbf(float x) { return (float)(__bf16)x; }
DEV float max3(float a, float b, float c) { float r; asm("v_max3_f32 %0, %1, %2, %3" : "=v"(r) : "v"(a), "v"(b), "v"(c)); return r; }
template <int J> DEV u32x4 piece4(f32x16 a) { return __builtin_bit_cast(u32x4, __builtin_shufflevector(a, a, 4 * J, 4 * J + 1, 4 * J + 2, 4 * J + 3)); }
DEV float relu(float x, int lo) { const int i = __float_as_int(x); return __int_as_float(i > lo ? i : lo); }
DEV u32x2 split3(float c) { const float a = rbf(c), b = rbf(c - a), d = c - a - b; return (u32x2){pk2(a, b), pk2(d, 0.f)}; }
template <int CTRL> DEV float dpp_add(float x) { return x + __int_as_float(__builtin_amdgcn_update_dpp(0, __float_as_int(x), CTRL, 0xf, 0xf, true)); }
DEV float swap_add32(float a, float b) { auto r = __builtin_amdgcn_permlane32_swap(__float_as_uint(a), __float_as_uint(b), false, false); return __uint_as_float(r[0]) + __uint_as_float(r[1]); }
DEV float swap_add16(float a, float b) { auto r = __builtin_amdgcn_permlane16_swap(__float_as_uint(a), __float_as_uint(b), false, false); return __uint_as_float(r[0]) + __uint_as_float(r[1]); }
DEV float swap_max32(float a) { auto r = __builtin_amdgcn_permlane32_swap(__float_as_uint(a), __float_as_uint(a), false, false); return max3(a, __uint_as_float(r[0]), __uint_as_float(r[1])); }
DEV u64 uni64(u32x2 v) { return ((u64)(unsigned)__builtin_amdgcn_readfirstlane(v[1]) << 32) | (unsigned)__builtin_amdgcn_readfirstlane(v[0]); }
constexpr u64 edge_mask(int r) { const int kk = (r & 3) + 8 * (r >> 2); return ((1ull << kk) - 1) | (((1ull << (kk + 4)) - 1) << 32); }

struct Buf { const void* p; unsigned bytes, mode; };
struct Params { Buf x, wqkv, wg, wu, ws, wd, wo, out; };

#define STAMP(i) do {} while (0)

constexpr unsigned SPIN_CAP = 200000;
DEV bool tagged(u32x4 v, u64 tag) { return (((v[0] ^ (unsigned)tag) | (v[2] ^ (unsigned)tag)) | ((v[1] ^ (unsigned)(tag >> 32)) | (v[3] ^ (unsigned)(tag >> 32)))) == 0u; }
DEV u32x4 poll(u32x4 ws, unsigned va, unsigned sa, u64 tag, bool need = true) {
  u32x4 a;
#pragma unroll 1
  for (unsigned spin = 0; spin < SPIN_CAP; ++spin) {
    ld16c<0>(a, va, ws, sa);
    wait_vm<0>(); PIN(a);
    if (__all(tagged(a, tag) || !need)) break;
    __builtin_amdgcn_s_sleep(POLL_SLEEP);
  }
  return a;
}
DEV void put_flag(u32x4 ws, unsigned off, u64 tag, int copies = 8) {
  const int lane = fresh_lane();
  st8c<0>((u32x2){(unsigned)tag, (unsigned)(tag >> 32)}, lane < copies ? lane * FL_REP : OOB, ws, off);
}

constexpr int U_RP = 22;
constexpr int imin(int a, int b) { return a < b ? a : b; }
constexpr int wunit(int I) { return I < 10 ? 2 * I + 1 : (I < 12 ? I + 10 : (I < 15 ? I + 12 : (I == 15 ? 23 : I + 11))); }
constexpr int windex(int u) { int I = 0; while (wunit(I) != (u < 20 ? u | 1 : u)) ++I; return I; }
constexpr int prev_unit(int j) { return j < 32 ? j - 9 : (j < 35 ? j - 8 : 23); }
constexpr unsigned upos(int k) { return k < 9 ? ((k + 8) % 9) * UNIT : upos(prev_unit(k)); }
static_assert(upos(27) == 8 * UNIT && upos(28) == 0 && upos(29) == UNIT && upos(30) == 2 * UNIT, "positions released first hold the attention image");
constexpr bool has_bar(int I) { return I <= 8 || I == 10 || I == 12 || I == 15; }
template <int K, int OFF, int NAP = 0> DEV void await(unsigned& v, unsigned addr) {
  unsigned n;
  asm volatile("v_cmp_ge_u32 vcc, %3, %0\n\ts_cbranch_vccz 2f\n\ts_mov_b32 %1, 0x200000\n1:\n\tds_read_b32 %0, %2 offset:%4\n\ts_sub_u32 %1, %1, 1\n\t"
               "s_waitcnt lgkmcnt(0)\n\ts_cbranch_scc1 2f\n\tv_cmp_ge_u32 vcc, %3, %0\n\ts_cbranch_vccz 2f\n\ts_sleep %5\n\ts_branch 1b\n2:"
               : "+v"(v), "=&s"(n) : "v"(addr), "i"(K), "i"(OFF), "i"(NAP) : "vcc", "scc", "memory");
}

DEV int perm6(int i) { return i ^ (6 * (((i + 2) >> 2) & 1)); }

#define ROLE_SETUP()                                                                                                                              \
  u32x16 ka, kb;                                                                                                                                  \
  asm volatile("s_load_dwordx16 %0, %2, 0x0\n\ts_load_dwordx16 %1, %2, 0x40" : "=&s"(ka), "=&s"(kb) : "s"(__builtin_amdgcn_kernarg_segment_ptr())); \
  int b = blockIdx.x; PINS(b);                                                                                                                    \
  const int xcd = b & 7, rr = b >> 3;                                                                                                             \
  const int p = (xcd >> 2) * 8 + (rr & 7), c = (xcd & 3) * 4 + (rr >> 3);                                                                         \
  const int tyB = c & 1, ci = c >> 1;                                                                                                             \
  const int ub = 3 * ci + 2 + tyB * (22 - 3 * ci + perm6(ci)), pg = 16 + ci - 8 * tyB;                                                                  \
  const int head = 4 * xcd + (rr & 3), qb = rr >> 2, par = head & 1, o0 = (5 * head) >> 1;                                                        \
  const int mt = MAP48 ? 4 * xcd + (rr & 3) : (xcd >> 1) * 8 + (rr & 7), jn = MAP48 ? rr >> 2 : (xcd & 1) * 4 + (rr >> 3);                                                                       \
  (void)0

__global__ __launch_bounds__(512, 2) void fused(Params P) {
#if defined(__HIP_DEVICE_COMPILE__)
  {
    unsigned c0, c1, len;
    asm volatile("s_getpc_b64 s[98:99]\n.Lpc_here:\n\ts_sub_u32 %0, s98, .Lpc_here-_Z5fused6Params\n\ts_subb_u32 %1, s99, 0\n\ts_mov_b32 %2, .Lcode_end-_Z5fused6Params"
                 : "=s"(c0), "=s"(c1), "=s"(len) :: "s98", "s99", "scc");
    const u32x4 srd_c = {c0, c1 & 0xffffu, len, 0x00020000u};
    asm volatile("buffer_load_dword v255, %0, %1, 0 offen" :: "v"(threadIdx.x * 2048 + (blockIdx.x & 120) * 16), "s"(srd_c) : "memory", "v255");
    SBAR();
  }
#define ARGS_READY() asm volatile("s_waitcnt lgkmcnt(0)" : "+s"(ka), "+s"(kb))
#define SRD(v, i) __builtin_shufflevector(v, v, 4 * (i), 4 * (i) + 1, 4 * (i) + 2, 4 * (i) + 3)
  __shared__ __attribute__((aligned(1024))) char smem[163840];
  const unsigned L0 = (unsigned)(uintptr_t)(LAS char*)smem;
  const int w = __builtin_amdgcn_readfirstlane(threadIdx.x >> 6);

  if (w < 4) {
    ROLE_SETUP();
    ARGS_READY();
    const u32x4 srd_ws = SRD(kb, 0), srd_x = SRD(ka, 0);
    const u32x4 srd_o = SRD(kb, 3);
    const int tb = 2 * p + (w >> 1);
    u64 tag;
    unsigned vo_st;
    unsigned ak0, ak1, av, aq, am, sy;
    bool posted;
    {
      constexpr int D = DEPTH;
      bf16x8 xf[40];
      s16x4 wl[D], wh[D];
      f32x4 acc[8][2];
      u32x4 pk[8];
      unsigned ax, aw;
      {
        const int lane = fresh_lane(), g = lane >> 4, q = (lane & 15) >> 2;
        ax = L0 + ((lane & 15) >> 1) * 1040 + (2 * w + (lane & 1)) * 128 + g * 16;
        aw = L0 + (q & 1) * 128 + (q >> 1) * 2080 + (g & 1) * 4160 + (g >> 1) * 512 + 8 * (lane & 3);
      }
      unsigned cur, cx;
      unsigned fw;
      {
        const int lane = fresh_lane();
        *(LAS unsigned*)(L0 + L_SY + w * 64 + (lane & 15) * 4) = 0u;
        asm volatile("s_waitcnt lgkmcnt(0)" ::: "memory");
      }
      u32x2 mf[2];
      u32x4 tab;
      float rstd[2];
      u32x2 ep;
      u32x4 xv[3];
      const int srow = p * 128 + 8 * c + 2 * w;
      auto stat_load = [&]() INL {
        const int lane = fresh_lane(), l31 = lane & 31;
        const unsigned vo = (lane >> 5) * 1280 + l31 * 16;
        __builtin_amdgcn_s_setprio(3);
        ld8c<FL_EPOCH>(ep, 0u, srd_ws, 0u);
        ld16<0>(xv[0], vo, srd_x, srow * 1280);
        ld16<512>(xv[1], vo, srd_x, srow * 1280);
        ld16<1024>(xv[2], l31 < 16 ? vo : OOB, srd_x, srow * 1280);
        __builtin_amdgcn_s_setprio(0);
      };
      auto stat_put = [&]() INL {
        wait_vm<0>();
        PIN(ep); PIN(xv[0]); PIN(xv[1]); PIN(xv[2]);
        tag = uni64(ep) + 1;
        float s1 = 0.f, s2 = 0.f;
        const bf16x2 one2 = __builtin_bit_cast(bf16x2, 0x3f803f80u);
#pragma unroll
        for (int i = 0; i < 12; ++i) {
          const unsigned raw = xv[i >> 2][i & 3];
          const bf16x2 v = __builtin_bit_cast(bf16x2, raw);
          s1 = __builtin_amdgcn_fdot2_f32_bf16(v, one2, s1, false);
          s2 = __builtin_amdgcn_fdot2_f32_bf16(v, v, s2, false);
        }
        s1 = dpp_add<0xb1>(s1); s2 = dpp_add<0xb1>(s2);
        s1 = dpp_add<0x4e>(s1); s2 = dpp_add<0x4e>(s2);
        s1 = dpp_add<0x141>(s1); s2 = dpp_add<0x141>(s2);
        s1 = dpp_add<0x140>(s1); s2 = dpp_add<0x140>(s2);
        s1 = swap_add16(s1, s1); s2 = swap_add16(s2, s2);
        const float mean = s1 * (1.f / DM), var = fmaxf(s2 * (1.f / DM) - mean * mean, 0.f);
        const unsigned pay = pk2(-mean, __builtin_amdgcn_sqrtf(rbf(rbf(var) + 1e-5f)));
        const int lane = fresh_lane(), l31 = lane & 31;
        const u32x2 gr = {((unsigned)tag << 16) | ((lane & 1) ? pay >> 16 : pay & 0xffffu), (unsigned)(tag >> 16)};
        __builtin_amdgcn_s_setprio(3);
        st8c<0>(gr, l31 < 2 ? (lane >> 5) * 16 + l31 * 8 : OOB, srd_ws, WS_STAT + srow * 16);
        __builtin_amdgcn_s_setprio(0);
      };
      constexpr int ZA = 168, ZB = 252, ZC = 420;
      constexpr auto z2f = [](int z) constexpr { return z < 160 ? z : (z < ZA ? -1 : (z < ZA + 80 ? z - 8 : (z < ZB ? -1 : (z < ZB + 160 ? z - 12 : -1)))); };
      constexpr auto zct = [](int z) constexpr { return z < ZA ? z - 160 : (z < ZB ? z - ZA - 80 : z - ZB - 160); };
      constexpr auto ctile = [](int z, int t) constexpr { return (z > 200 && z < 300) ? t : (t >> 1) + 4 * (t & 1); };
      constexpr auto woff = [](int wu, int i) constexpr {
        return (wu >= 10 && wu < 15) ? (i >> 3) * 8320 + ((i >> 2) & 1) * 1024 + (i & 3) * 32 : (i >> 3) * 1024 + ((i >> 2) & 1) * 8320 + (i & 3) * 32;
      };
      auto rd_lo = [&](auto Z_) {
        constexpr int z = Z_, f = z2f(z);
        if constexpr (f < 0) {
          constexpr int t = zct(z);
          constexpr bool beta = z > 200 && z < 300;
          if constexpr (t == 0) {
            const int lane = fresh_lane();
            cur = L0 + L_E + (lane & (beta ? 63 : 31)) * 8; PIN(cur);
            cx = L0 + L_ST + w * 256 + (lane & 15) * 8; PIN(cx);
            if constexpr (z < 400) bar(); else await<0, SY_EV>(fw, sy);
            mf[0] = *(const LAS u32x2*)cx; mf[1] = *(const LAS u32x2*)(cx + 128);
          }
          wl[z % D] = *(const LAS s16x4*)(cur + ctile(z, t) * (beta ? 512 : 256));
        } else {
          constexpr int wu = f >> 4, i = f & 15, k = wunit(wu);
          if constexpr (i == 0) {
            PIN(aw); cur = aw + upos(k); PIN(cur);
            if constexpr (wu < 10) { PIN(ax); cx = ax + upos(k - 1); PIN(cx); }
            if constexpr (wu == 2) { SBAR(); stat_put(); SBAR(); }
            if constexpr (has_bar(wu) && wu != 10 && wu != 15) bar();
            if constexpr (wu == 19 || wu == 21) await<(wu == 21), SY_FULL>(fw, sy);
          }
          if constexpr (i == D && (wu == 16 || wu == 18 || wu == 20)) *(LAS unsigned*)(sy + SY_DONE) = wu;
          if constexpr (wu < 10 && (i == 0 || i == 4)) xf[4 * wu + (i >> 1)] = lds_b128(cx + 16 * i);
          wl[z % D] = lds_tr(cur + woff(wu, i));
        }
      };
      auto rd_hi = [&](auto Z_) {
        constexpr int z = Z_, f = z2f(z);
        if constexpr (f >= 0) {
          constexpr int wu = f >> 4, i = f & 15;
          wh[z % D] = lds_tr(cur + woff(wu, i) + 256);
          if constexpr (wu < 10 && (i == 0 || i == 6)) xf[4 * wu + (i ? 3 : 1)] = lds_b128(cx + 8320 + (i ? 64 : 0));
          if constexpr (i == 11 && (wu == 18 || wu == 20 || wu == 24)) fw = *(const LAS unsigned*)(sy + (wu == 24 ? SY_EV : SY_FULL));
          if constexpr (i == 2 && wu == 16) { sy = L0 + L_SY + w * 64 + (fresh_lane() & 3) * 4; PIN(sy); }
        }
      };
      auto pack = [&](f32x4 a, f32x4 b) INL {
        const auto r0 = __builtin_amdgcn_permlane16_swap(pk2(a[0], a[1]), pk2(b[0], b[1]), false, false);
        const auto r1 = __builtin_amdgcn_permlane16_swap(pk2(a[2], a[3]), pk2(b[2], b[3]), false, false);
        return (u32x4){r0[0], r1[0], r0[1], r1[1]};
      };
      auto relu4 = [&](f32x4 a, int lo = 0) INL { return (f32x4){relu(a[0], lo), relu(a[1], lo), relu(a[2], lo), relu(a[3], lo)}; };
      auto epi = [&](auto PASS_) {
        constexpr int pass = PASS_;
        constexpr float QS = 0.22360679774997896f * 1.4426950408889634f;
        mma_settle();
        SBAR();
        const int lane = fresh_lane();
        vo_st = (lane >> 5) * 1024 + (32 * (w & 1) + (lane & 31)) * 16;
        if constexpr (pass == 0) {
          const float q0 = tyB ? 0.f : (3 * ci < 10 ? QS : 1.f), q1 = 3 * ci + 1 < 10 ? QS : 1.f, kB = tyB ? 1.f : 0.f;
          const float u0 = rstd[0] * (tyB ? rstd[0] : q1), u1 = rstd[1] * (tyB ? rstd[1] : q1), a0 = rstd[0] * q0, a1 = rstd[1] * q0;
          const int lo = tyB ? 0 : (int)0x80000000;
#pragma unroll
          for (int cb = 0; cb < 4; ++cb) {
            const f32x4 y0 = acc[cb + 4][0] * u0, y1 = acc[cb + 4][1] * u1;
            pk[2 * cb] = pack(relu4(acc[cb][0], lo) * __builtin_elementwise_fma(y0, (f32x4)kB, (f32x4)a0), relu4(acc[cb][1], lo) * __builtin_elementwise_fma(y1, (f32x4)kB, (f32x4)a1));
            pk[2 * cb + 1] = pack(y0, y1);
            PIN(pk[2 * cb]); PIN(pk[2 * cb + 1]);
            SBAR();
            if (cb == 2) { static_for<ZA, ZA + D - 1>([&](auto Z_) { rd_lo(Z_); rd_hi(Z_); }); SBAR(); }
          }
        } else {
          __builtin_amdgcn_s_setprio(3);
          const float qs = ub < 10 ? QS : 1.f;
          const float s0 = rstd[0] * (pass == 1 ? qs : rstd[0]), s1 = rstd[1] * (pass == 1 ? qs : rstd[1]);
          const unsigned vq = (pass == 2 || ub < 30) ? vo_st : OOB;
          const unsigned so = pass == 1 ? WS_QKV + tb * QKV_TB + (ub < 30 ? ub : 0) * 8192 : WS_A2 + tb * A2_MT + pg * 8192;
#pragma unroll
          for (int cb = 0; cb < 4; ++cb) {
            const u32x4 v = pass == 1 ? pack(acc[cb][0] * s0, acc[cb][1] * s1) : pack(relu4(acc[cb][0]) * (acc[cb + 4][0] * s0), relu4(acc[cb][1]) * (acc[cb + 4][1] * s1));
            st16c<0>(v, vq, srd_ws, so + cb * 2048);
            SBAR();
          }
          __builtin_amdgcn_s_setprio(pass == 1 ? 0 : 1);
        }
      };
      const unsigned so_al = tyB ? WS_A2 + tb * A2_MT + ci * 8192 : WS_QKV + tb * QKV_TB + 3 * ci * 8192;
      u32x4 srd_hi = srd_ws;
      srd_hi[2] = tyB ? 0u : srd_ws[2];
      auto trickle = [&](auto J_) {
        constexpr int j = J_;
        st16p<(j >> 1) * 2048 + (j & 1) * 8192>(pk[j], vo_st, (j & 1) ? srd_hi : srd_ws, so_al);
      };
      static_for<0, D - 1>([&](auto Z_) { rd_lo(Z_); rd_hi(Z_); });
      STAMP(1);
      auto mm = [&](auto Z_, auto T_) {
        constexpr int z = Z_, tt = T_, f = z2f(z);
        if constexpr (f < 0) {
          constexpr int t = ctile(z, zct(z));
          if constexpr (zct(z) == 0) {
            rstd[tt] = __uint_as_float(mf[tt][1]); mf[tt][1] = mf[tt][0] & 0xffffu;
            asm volatile("s_nop 1" : "+v"(mf[tt]), "+v"(rstd[tt]));
          }
          mma16k16(acc[t][tt], wl[z % D], mf[tt]);
        } else {
          constexpr int wu = f >> 4, i = f & 15;
          constexpr bool beta = wu >= 10 && wu < 15;
          constexpr int t = beta ? (i & 3) : (i & 7);
          constexpr int ks = wu < 10 ? 2 * wu + (i >> 3) : (beta ? 4 * (wu - 10) + (i >> 2) : 2 * (wu - 15) + (i >> 3));
          mma16<ks == 0>(acc[t][tt], join(wl[z % D], wh[z % D]), xf[2 * ks + tt]);
        }
        SBAR();
      };
      auto slot = [&](auto Z_) {
        constexpr int z = Z_, f = z2f(z);
        constexpr bool ahead = z + D - 1 < ZC && !(z < ZA && z + D - 1 >= ZA);
        if constexpr (ahead) rd_lo(IC<z + D - 1>{});
        SBAR();
        mm(Z_, IC<0>{});
        if constexpr (ahead) rd_hi(IC<z + D - 1>{});
        SBAR();
        mm(Z_, IC<1>{});
        if constexpr (FINE == 4 && f >= 0 && (f & 15) == 8) STAMP(8 + (f >> 4));
        if constexpr (z == 10) { stat_load(); SBAR(); }
        if constexpr (z == ZA - 1) { STAMP(44); epi(IC<0>{}); STAMP(2); SBAR(); }
        if constexpr (z >= ZA + 2 && z < ZA + 2 + 8 * 9 && (z - ZA - 2) % 9 == 0) { trickle(IC<(z - ZA - 2) / 9>{}); SBAR(); }
        if constexpr (z == ZB - 1) { STAMP(45); epi(IC<1>{}); STAMP(3); SBAR(); }
        if constexpr (z == ZB - 1 + ZF1) {
          STAMP(47);
          wait_vm<0>();
          __builtin_amdgcn_s_setprio(3); put_flag(srd_ws, FL_1 + (tb * 32 + 2 * c + (w & 1)) * 8, tag); __builtin_amdgcn_s_setprio(0);
          STAMP(48);
          SBAR();
        }
        if constexpr (z == ZC - 11) { am = fresh_lane() * 16; tab = *(const LAS u32x4*)(L0 + L_T + am); SBAR(); }
        if constexpr (z == ZC - 5) { ak0 = tab[0] + w * 1024; ak1 = tab[1] + w * 1024; PIN(ak0); PIN(ak1); SBAR(); }
        if constexpr (z == ZC - 4) { av = tab[2] + w * 1024; aq = tab[3] + w * 1024; PIN(av); PIN(aq); SBAR(); }
        if constexpr (z == ZC - 3) { am += L0 + L_MK; PIN(am); SBAR(); }
        if constexpr (z == ZC - 1) {
          STAMP(46); epi(IC<2>{}); STAMP(4);
          *(LAS unsigned*)(sy + SY_DONE) = 25u;
          fw = *(const LAS unsigned*)(sy + SY_EV);
          posted = !__all(fw > 1u);
          if (posted) { wait_vm<0>(); put_flag(srd_ws, FL_2 + (tb * 32 + 2 * c + (w & 1)) * 8, tag); }
          await<1, SY_EV>(fw, sy);
          SBAR();
        }
      };
      static_for<0, 210>(slot);
      static_for<210, ZC>(slot);
    }
    STAMP(5);
    {
      u32x4 q0[2], q1[2];
      f32x16 o[2];
      {
        constexpr auto ktile = [](int k) constexpr { return k < 16 ? (k + 3) >> 1 : k & 1; };
        f32x16 S[2];
        f32x4 mk[4];
        bf16x8 kf[2], kg[2];
        s16x4 vf[4];
        u32x4 pa[2], pb[2];
        unsigned fe;
        q0[0] = *(const LAS u32x4*)aq; kg[0] = lds_b128(ak0 + 512);
        q1[0] = *(const LAS u32x4*)(aq + 8192); kg[1] = lds_b128(ak1 + 512);
        q0[1] = *(const LAS u32x4*)(aq + 512); kf[0] = lds_b128(ak0 + 1024);
        q1[1] = *(const LAS u32x4*)(aq + 8192 + 512); kf[1] = lds_b128(ak1 + 1024);
        SBAR();
        S[0] = mfma32(kg[0], asbf(q0[0]), (f32x16){});
        S[0] = mfma32(kg[1], asbf(q1[0]), S[0]);
        SBAR();
        float e[2][4];
        auto cvt = [&](auto X_, auto H_) {
          constexpr int x = X_, h = H_, k = x >> 2, gq = x & 3;
          if constexpr (x >= 0) {
            u32x4& d = gq < 2 ? pa[k & 1] : pb[k & 1];
            d[2 * (gq & 1) + h] = pk2(e[x & 1][2 * h], e[x & 1][2 * h + 1]);
            PIN(d);
            SBAR();
          }
        };
        auto grp = [&](auto K_, auto G_) {
          constexpr int k = K_, gq = G_;
          static_for<0, 4>([&](auto J_) {
            constexpr int j = J_;
            float& ej = e[gq & 1][j];
            ej = __builtin_amdgcn_exp2f(S[k & 1][4 * gq + j]);
            SBAR();
            if constexpr (gq == 1 && j < 2 && k < 16 && ktile(k + 2) != ktile(k + 1)) kf[j] = lds_b128((j ? ak1 : ak0) + ktile(k + 2) * 512);
            if constexpr (gq == 3 && (k == 0 || ktile(k) != ktile(k - 1))) vf[j] = lds_tr(av + ktile(k) * 512 + j * 128);
            if constexpr (gq == 2 && (k == 12 || k == 14)) mk[j] = *(const LAS f32x4*)(am + (k == 12 ? 4096 : 0) + j * 1024);
            if constexpr (gq == 0 && j == 0 && k == 11) fe = *(const LAS unsigned*)(sy + SY_EV);
            SBAR();
            if constexpr (j & 1) cvt(IC<4 * k + gq - 1>{}, IC<(j >> 1)>{});
          });
        };
        static_for<0, 19>([&](auto K_) {
          constexpr int k = K_, n = k + 1, pv = k - 1;
          if constexpr (k == KF2) { if (!posted) { wait_vm<0>(); put_flag(srd_ws, FL_2 + (tb * 32 + 2 * c + (w & 1)) * 8, tag); } SBAR(); }
          if constexpr (k == 12) { await<2, SY_EV>(fe, sy); SBAR(); }
          if constexpr (FINE == 3 && (k & 1) == 0 && k < 18) STAMP(8 + k);
          f32x16 sn;
          if constexpr (n < 18) {
            f32x16 c0 = {};
            if constexpr (n >= 14) c0 = __builtin_shufflevector(__builtin_shufflevector(mk[0], mk[1], 0, 1, 2, 3, 4, 5, 6, 7), __builtin_shufflevector(mk[2], mk[3], 0, 1, 2, 3, 4, 5, 6, 7),
                                                                0, 1, 2, 3, 4, 5, 6, 7, 8, 9, 10, 11, 12, 13, 14, 15);
            sn = mfma32(kf[0], asbf(q0[n & 1]), c0); PIN(sn); SBAR();
          }
          if constexpr (k < 18) grp(K_, IC<0>{});
          if constexpr (n < 18) { sn = mfma32(kf[1], asbf(q1[n & 1]), sn); PIN(sn); SBAR(); }
          if constexpr (k < 18) grp(K_, IC<1>{});
          if constexpr (pv >= 0) { o[pv & 1] = mfma32(join(vf[0], vf[1]), asbf(pa[pv & 1]), pv < 2 ? (f32x16){} : o[pv & 1]); PIN(o[pv & 1]); SBAR(); }
          if constexpr (k < 18) grp(K_, IC<2>{});
          if constexpr (k == 18) { cvt(IC<71>{}, IC<0>{}); cvt(IC<71>{}, IC<1>{}); }
          if constexpr (pv >= 0) { o[pv & 1] = mfma32(join(vf[2], vf[3]), asbf(pb[pv & 1]), o[pv & 1]); PIN(o[pv & 1]); SBAR(); }
          if constexpr (k < 18) grp(K_, IC<3>{});
          if constexpr (n < 18) S[n & 1] = sn;
        });
      }
      if constexpr (FINE == 3) STAMP(26);
      const int lane = fresh_lane(), l31 = lane & 31, hh = lane >> 5;
      const unsigned so = WS_A2 + (4 * qb + w) * A2_MT;
      auto put_o = [&](int a) INL {
        const float il = __builtin_amdgcn_rcpf(o[a][12]);
        st16c<0>((u32x4){pk2(o[a][0] * il, o[a][1] * il), pk2(o[a][2] * il, o[a][3] * il), pk2(o[a][4] * il, o[a][5] * il), pk2(o[a][6] * il, o[a][7] * il)},
                 hh * 1024 + l31 * 16, srd_ws, so + (192 + 2 * head) * 1024 + a * 512);
        st8c<0>((u32x2){pk2(o[a][8] * il, o[a][9] * il), pk2(o[a][10] * il, o[a][11] * il)}, hh ? OOB : l31 * 8, srd_ws,
                so + 256 * 1024 + head * 512 + a * 256);
      };
      __builtin_amdgcn_s_setprio(3);
      put_o(0);
      SBAR();
      put_o(1);
      SBAR();
      if (!__all(o[0][12] < 1e18f && o[1][12] < 1e18f && o[0][12] > 1e-18f && o[1][12] > 1e-18f)) {
#pragma unroll
        for (int a = 0; a < 2; ++a) {
          q1[a][0] = hh ? q1[a][0] & 0xffffu : q1[a][0];
          float mx = -__builtin_inff();
          o[a] = (f32x16){};
#pragma unroll 1
          for (int it = 0; it < 18; ++it) {
            const int i = it < 9 ? it : it - 9;
            f32x16 t = mfma32(lds_b128(ak0 + (a + i) * 512), asbf(q0[a]), (f32x16){});
            t = mfma32(lds_b128(ak1 + (a + i) * 512), asbf(q1[a]), t);
#pragma unroll
            for (int r = 0; r < 16; ++r) {
              const unsigned dist = 256 + l31 - 32 * i - ((r & 3) + 8 * (r >> 2) + 4 * hh);
              t[r] = dist < 256u ? t[r] : -__builtin_inff();
            }
            if (it < 9) {
#pragma unroll
              for (int r = 0; r < 16; r += 2) mx = max3(mx, t[r], t[r + 1]);
              if (it == 8) mx = swap_max32(mx);
            } else {
              u32x4 p0, p1;
#pragma unroll
              for (int r = 0; r < 4; ++r) {
                p0[r] = pk2(__builtin_amdgcn_exp2f(t[2 * r] - mx), __builtin_amdgcn_exp2f(t[2 * r + 1] - mx));
                p1[r] = pk2(__builtin_amdgcn_exp2f(t[8 + 2 * r] - mx), __builtin_amdgcn_exp2f(t[9 + 2 * r] - mx));
              }
              const unsigned vb = av + (a + i) * 512;
              o[a] = mfma32(join(lds_tr(vb), lds_tr(vb + 128)), asbf(p0), o[a]);
              o[a] = mfma32(join(lds_tr(vb + 256), lds_tr(vb + 384)), asbf(p1), o[a]);
            }
          }
          wait_vm<0>();
          put_o(a);
        }
      }
    }
    STAMP(6);
    {
      const int lane = fresh_lane();
      const unsigned l16 = lane * 16;
      unsigned sya = L0 + L_SY + (lane & 3) * 64, dn = 0u;
      PIN(sya);
      *(LAS unsigned*)(sy + SY_DONE) = 26u;
      wait_vm<0>();
      put_flag(srd_ws, FL_3 + ((4 * qb + w) * 32 + head) * 8, tag);
      STAMP(35);
      await<25, SY_DONE, 1>(dn, sya);
      const unsigned so_w = WS_R + jn * R_J + 48 * 5120;
      dma16c_x4<w_slot(72)>(L0 + w * 4096, l16, srd_ws, so_w + 72 * 1024 + w * 4096);
      static_for<0, 3>([&](auto J_) { dma16c<w_slot(88) + J_ * 1024, (88 + J_) * 1024>(L0 + w * 3072, l16, srd_ws, so_w + w * 3072); });
      u32x4 xr[3];
      unsigned go[3], ra[3];
      const unsigned so_out = (mt * 64 + 16 * w) * 1280 + jn * 160;
      const unsigned ap = (w < 3 ? L0 + L_P0 + w * 20480 : L0 + L_P3);
#pragma unroll
      for (int j = 0; j < 3; ++j) {
        const int id = lane + 64 * j, row = id / 10;
        go[j] = id < 160 ? (id + 70 * row) * 16 : OOB;
        ra[j] = ap + id * 32 + row * (PROW - 320);
        PIN(ra[j]);
        ld16<0>(xr[j], go[j], srd_x, so_out);
      }
      unsigned ev = 0u;
      await<3, SY_EV>(ev, sy);
      bf16x8 wr[NWR];
      unsigned aw = L0 + l16, aw1 = aw + L_W0, aw2 = aw + L_W0 + 48 * 1024;
      PIN(aw); PIN(aw1); PIN(aw2);
#pragma unroll
      for (int j = 0; j < NWR; ++j) { wr[j] = lds_b128(aw1 + j * 1024); if (j % 14 == 13) SBAR(); }
#pragma unroll
      for (int j = 0; j < NWR; ++j) PIN(wr[j]);
      *(LAS unsigned*)(sy + SY_DONE) = 27u;
      STAMP(37);
      if (w == 0) {
        __builtin_amdgcn_s_sleep(O_SLEEP);
        poll(srd_ws, l16 & 255, jn * FL_REP + FL_3 + mt * 256, tag);
        *(LAS unsigned*)(L0 + L_SY + SY_CNT) = 1u;
      } else {
        unsigned v = 0u, a = L0 + L_SY;
        PIN(a);
        await<0, SY_CNT, 1>(v, a);
      }
      STAMP(38);
      u32x4 of[16];
      u32x2 ot[8];
      {
        const unsigned so_o = WS_A2 + mt * A2_MT + 192 * 1024, va = (lane >> 4) * 1024 + (lane & 15) * 16, vt = (lane >> 4) * 1024 + (lane & 15) * 8;
        static_for<0, 16>([&](auto K_) { ld16c<0>(of[K_], va, srd_ws, so_o + w * 256 + K_ * 4096); });
        static_for<0, 8>([&](auto K_) { ld8c<(K_ & 1) * 512>(ot[K_], vt, srd_ws, so_o + 65536 + w * 128 + (K_ >> 1) * 4096); });
      }
      __builtin_amdgcn_s_setprio(0);
      wait_vm<24>();
      PIN(xr[0]); PIN(xr[1]); PIN(xr[2]);
      *(LAS unsigned*)(sy + SY_DONE) = 28u;
      f32x4 dsum[5], acc[5];
      auto down = [&]() INL {
#pragma unroll
        for (int t = 0; t < 5; ++t) {
          const unsigned a = ap + l16 + t * 1024;
          dsum[t] = (*(const LAS f32x4*)a + *(const LAS f32x4*)(a + 5120)) + (*(const LAS f32x4*)(a + 10240) + *(const LAS f32x4*)(a + 15360));
        }
      };
      ev = *(const LAS unsigned*)(sy + SY_EV);
      const bool dearly = __all(ev > 4u);
      asm volatile("" ::: "memory");
      if (dearly) down();
      STAMP(39);
      constexpr int DW = 6;
      bf16x8 wq[DW];
      static_for<0, 100>([&](auto J_) {
        constexpr int j = J_, ks = j / 5, t = j % 5, jn2 = j + DW - 1;
        if constexpr (jn2 == 72) await<27, SY_DONE>(dn, sya);
        if constexpr (jn2 >= NWR && jn2 < 100)
          wq[jn2 % DW] = jn2 < 48 ? lds_b128(aw1 + jn2 * 1024) : (jn2 < 88 ? lds_b128(aw2 + (w_slot(jn2) - L_W0 - 48 * 1024)) : lds_b128(aw + w_slot(jn2)));
        if constexpr (j == 55) dn = *(const LAS unsigned*)(sya + SY_DONE);
        if constexpr (t == 0) {
          wait_vm<(ks < 16 ? 23 - ks : 38 - 2 * ks)>();
          if constexpr (ks >= 16) {
            u32x2& lo = ot[2 * (ks - 16)]; u32x2& hi = ot[2 * (ks - 16) + 1];
            PIN(lo); PIN(hi);
            of[ks - 16] = (u32x4){lo[0], lo[1], hi[0], hi[1]};
            u32x4& m = of[ks - 16];
            asm volatile("s_nop 1" : "+v"(m));
          }
        }
        SBAR();
        mma16<ks == 0>(acc[t], j < NWR ? wr[j < NWR ? j : 0] : wq[j % DW], asbf(of[ks & 15]));
        SBAR();
      });
      STAMP(40);
      mma_settle();
      SBAR();
      if (!dearly) { await<4, SY_EV>(ev, sy); down(); }
      const unsigned at = ap + (lane & 15) * PROW + (lane >> 4) * 16;
#pragma unroll
      for (int t = 0; t < 5; ++t) { acc[t] += dsum[t]; PIN(acc[t]); }
#pragma unroll
      for (int t = 0; t < 5; ++t) *(LAS f32x4*)(at + 64 * t) = acc[t];
#pragma unroll
      for (int j = 0; j < 3; ++j) {
        const f32x4 lo = *(const LAS f32x4*)ra[j], hi = *(const LAS f32x4*)(ra[j] + 16);
        const u32x4 xv = xr[j];
        u32x4 ov;
        ov[0] = pk2(lo[0] + __uint_as_float(xv[0] << 16), lo[1] + __uint_as_float(xv[0] & 0xffff0000u));
        ov[1] = pk2(lo[2] + __uint_as_float(xv[1] << 16), lo[3] + __uint_as_float(xv[1] & 0xffff0000u));
        ov[2] = pk2(hi[0] + __uint_as_float(xv[2] << 16), hi[1] + __uint_as_float(xv[2] & 0xffff0000u));
        ov[3] = pk2(hi[2] + __uint_as_float(xv[3] << 16), hi[3] + __uint_as_float(xv[3] & 0xffff0000u));
        st16<0>(ov, go[j], srd_o, so_out);
      }
      STAMP(7);
    }
  } else {
    ROLE_SETUP();
    __builtin_amdgcn_s_setprio(2);
    const int s = w - 4;
    const int lane = fresh_lane();
    const int rho = lane >> 3;
    unsigned vo_x = (32 * (rho >> 1) + (rho & 1)) * 1280 + (lane & 7) * 16;
    const int wrow = (rho & 1) + 4 * ((rho >> 1) & 1) + 16 * (rho >> 2);
    unsigned vo_q = wrow * 3840 + (lane & 7) * 16;
    unsigned vo_g = wrow * 3072 + (lane & 7) * 16;
    unsigned st_al = 3840 - 768 * tyB;
    unsigned vo_al[4];
#pragma unroll
    for (int m = 0; m < 4; ++m) { vo_al[m] = (tyB ? vo_g : vo_q) + (2 * (m & 1) + 32 * (m >> 1)) * st_al; PIN(vo_al[m]); }
    unsigned c_x = (p * 128 + 8 * s) * 1280, c_al = (ci + (1 - tyB) * (2 * ci + (s >> 1))) * 128 + 8 * (s & 1) * st_al;
    unsigned d_x = L0 + s * 4160;
    int rot = ((10 * (c & 3) + 5 * (p & 7)) >> 2) + RSH * (p >> 3);
    PINS(rot);
    rot = rot >= 10 ? rot - 10 : rot;
    PIN(vo_x); PINS(c_x); PINS(c_al); PINS(d_x); PINS(rot); PINS(st_al);
    SBAR();
    ARGS_READY();
    const u32x4 srd_x = SRD(ka, 0), srd_ws = SRD(kb, 0);
    const u32x4 srd_gu = (s >> 1) ? SRD(ka, 3) : SRD(ka, 2);
    const u32x4 srd_al = tyB ? srd_gu : SRD(ka, 1);
    u32x4 srd_be, srd_rp;
    unsigned c_be, c_ga, vo_rp, so_rp[4];
    auto kst = [&](int n) INL { int k = n + rot; PINS(k); return k >= 10 ? k - 10 : k; };
    unsigned so;
    auto piece = [&](auto Q_) {
      constexpr int k = Q_ >> 2, m = Q_ & 3;
      constexpr unsigned d = upos(k);
      constexpr int wi = k == U_RP ? 0 : windex(k), mr = 2 * (m & 1) + 32 * (m >> 1);
      constexpr unsigned dw = d + (m & 1) * 2080 + (m >> 1) * 1024;
      if constexpr (k < 20 && !(k & 1)) {
        if constexpr (m == 0) so = c_x + kst(k >> 1) * 128;
        dma16<d + m * 1040, m * 2560>(d_x, vo_x, srd_x, so);
      } else if constexpr (k < 20) {
        if constexpr (m == 0) so = c_al + kst(k >> 1) * 64 * st_al;
        dma16<dw, 0>(d_x, vo_al[m], srd_al, so);
      } else if constexpr (k == U_RP) {
        dma16<d + m * 1024, 0>(d_x, vo_rp, srd_rp, so_rp[m]);
      } else if constexpr (wi < 15) {
        if constexpr (m == 0) so = c_be + kst(2 * (wi - 10) + (s >> 1)) * (64 * 3840);
        dma16<dw, mr * 3840>(d_x, vo_q, srd_be, so);
      } else {
        if constexpr (m == 0) so = c_ga + kst(wi - 15) * (64 * 3072);
        dma16<dw, mr * 3072>(d_x, vo_g, srd_gu, so);
      }
    };
    STAMP(60);
    u32x2 ep;
    static_for<0, 4>(piece);
    ld8c<FL_EPOCH>(ep, 0u, srd_ws, 0u);
    static_for<4, 16>(piece);
    SBAR();

    srd_be = SRD(ka, 1); srd_be[2] = ub < 30 ? srd_be[2] : 0u;
    c_be = (ub < 30 ? ub : 0) * 128 + 8 * (s & 1) * 3840; c_ga = pg * 128 + 8 * (s & 1) * 3072;
    const int ract = (s < 3) & (b * 3 + s < 680);
    const int ru = ract * (b * 3 + s), rs = ru / 10, rbl = ru - 10 * rs;
    {
      const int dn = rs < 48, tl = rs >= 64;
      srd_rp = dn ? SRD(kb, 1) : SRD(kb, 2);
      vo_rp = ract ? (tl ? 20 * (rho >> 2) + (rho & 3) : rho) * 1280 + (lane & 7) * 16 : OOB;
      const int r0 = rs * (40 - 8 * dn + 120 * tl) - 1920 * (1 - dn) - 8304 * tl;
      const int d1 = 8 + 32 * tl, d2 = 20 - 4 * dn + 60 * tl;
#pragma unroll
      for (int m = 0; m < 4; ++m) { so_rp[m] = (r0 + (m & 1) * d1 + (m >> 1) * d2) * 1280 + rbl * 128; PINS(so_rp[m]); }
      PIN(vo_rp); PINS(srd_rp);
    }
    const unsigned l16 = lane * 16, d_s = L0 + s * 1024;
    const int l31 = lane & 31, hh = lane >> 5;
    u64 tag = 0;
    {
      const int G = ((lane & 16) ? 4 : ((lane & 1) << 1) | ((lane >> 1) & 1)) + par;
      const unsigned tk = L0 + l31 * 16;
      const unsigned av = ((lane & 18) == 18 ? L_FR + 8 : (3 + (G >> 1)) * AROW + 8 * (G & 1)) + L0 + (4 * hh + ((lane & 15) >> 2)) * 16;
      *(LAS u32x4*)(L0 + L_T + l16) = (u32x4){tk + hh * AROW, tk + (hh ? L_FR : L_K2), av, tk + L_Q + hh * 4096};
    }
    const unsigned selw = ((lane & 15) >> 2) == (lane >> 4) ? 0x3f803f80u : 0u;
    const bf16x8 sel = asbf((u32x4){selw, selw, selw, selw});
    const unsigned lcs = ((lane & 15) >> 2) * 128 + (lane >> 4) * 32 + 8 * (lane & 3);
    const int gap = ((10 * (c & 3) + 5 * (p & 7) + 5) >> 2) - ((10 * (c & 3) + 5 * (p & 7)) >> 2) == 2;
    const unsigned so_cs = WS_CS + ((p >> 3) * 16 + c) * CS_SET + s * 5120;
    bf16x8 cf[4];
    auto cs_read = [&](auto K_) {
      constexpr int k = K_;
      unsigned ac = d_x + upos(k) + lcs; PIN(ac);
#pragma unroll
      for (int i = 0; i < 4; ++i) cf[i] = join(lds_tr(ac + (i & 1) * 2080 + (i >> 1) * 1024), lds_tr(ac + (i & 1) * 2080 + (i >> 1) * 1024 + 512));
    };
    auto cs_add = [&](f32x4& cs) INL {
#pragma unroll
      for (int i = 0; i < ((ABL & 1) ? 0 : 4); ++i) cs = mfma16(sel, cf[i], cs);
      PIN(cs);
      SBAR();
    };
    auto put_e = [&](bool beta, int col, float v) INL {
      const int e = beta ? (col >> 4) * 64 + 16 * s + (col & 15) : (4 * (s >> 1) + (col >> 4)) * 32 + 16 * (s & 1) + (col & 15);
      *(LAS u32x2*)(L0 + L_E + e * 8) = split3(beta ? v : 0.5f * v);
    };
    const unsigned sy = L0 + L_SY + (lane & 3) * 64;
    unsigned dn = 0u;
    unsigned syw = sy + 4 * s;
    PIN(syw);
    auto post = [&](auto OFF_, unsigned v) INL {
      asm volatile("s_waitcnt lgkmcnt(0)" ::: "memory");
      *(LAS unsigned*)(syw + OFF_) = v;
      asm volatile("" ::: "memory");
    };
    auto full = [&](unsigned v) INL { post(IC<SY_FULL>{}, v); };
    auto event = [&](unsigned v) INL { post(IC<SY_EV>{}, v); };
    u32x4 stg;
    f32x4 csa = {}, csb = {};
    static_for<0, 9>([&](auto N_) {
      constexpr int n = N_;
      constexpr int e0 = n == 0 ? 4 : (n == 1 ? 7 : 2 * n + 6), e1 = n == 0 ? 7 : 2 * n + 8;
      if constexpr (FINE == 1) STAMP(8 + n);
      wait_all<(n < 8 ? 4 * (e0 - 2 * n - 2) : 9)>();
      cs_read(IC<2 * n + 1>{});
      bar();
      if constexpr (FINE == 1) STAMP(35 + n);
      if constexpr (n == 7) ld16c<0>(stg, l31 * 16, srd_ws, WS_STAT + (p * 128 + 32 * s) * 16);
      piece(IC<4 * e0>{});
      cs_add(csa);
      if constexpr (n == 8) { cs_read(IC<19>{}); piece(IC<4 * e0 + 1>{}); cs_add(csa); }
      static_for<4 * e0 + 1 + (n == 8), 4 * e1>(piece);
      SBAR();
    });
    PIN(ep); tag = uni64(ep) + 1;
    put_e(false, lane, csa[0]);
    wait_vm<16>();
    PIN(stg);
    {
      const unsigned tl = (unsigned)tag << 16, th = (unsigned)(tag >> 16);
#pragma unroll 1
      for (unsigned spin = 0; spin < SPIN_CAP; ++spin) {
        if (__all(((stg[0] ^ tl) | (stg[2] ^ tl)) < 65536u && stg[1] == th && stg[3] == th)) break;
        __builtin_amdgcn_s_sleep(POLL_SLEEP);
        ld16c<0>(stg, l31 * 16, srd_ws, WS_STAT + (p * 128 + 32 * s) * 16);
        wait_vm<0>();
        PIN(stg);
      }
      *(LAS u32x2*)(L0 + L_ST + (32 * s + l31) * 8) = (u32x2){(stg[0] & 0xffffu) | (stg[0] << 16), __float_as_uint(__builtin_amdgcn_rcpf(__uint_as_float(stg[2] << 16)))};
    }
    if constexpr (FINE == 1) STAMP(18);
    wait_all<8>();
    cs_read(IC<20>{});
    bar();
    if constexpr (FINE == 1) STAMP(45);
    static_for<4 * 24, 4 * 25>(piece);
    cs_add(csb);
    cs_read(IC<21>{});
    static_for<4 * 25, 4 * 26>(piece);
    cs_add(csb);
    static_for<4 * 26, 4 * 28>(piece);
    wait_vm<20>();
    {
      const unsigned ar = d_x + upos(U_RP) + (8 * (lane >> 4) + ((lane & 15) >> 2)) * 128 + 8 * (lane & 3);
      const unsigned vs = ract ? l16 : OOB;
#pragma unroll
      for (int t = 0; t < 4; ++t) {
        const int ct = 4 * rbl + t, cj = (ct * 13) >> 6;
        const bf16x8 f = join(lds_tr(ar + 32 * t), lds_tr(ar + 32 * t + 512));
        st16c<0>(__builtin_bit_cast(u32x4, f), vs, srd_ws, WS_R + ((cj * 68 + rs) * 5 + ct - 5 * cj) * 1024);
      }
    }
    if constexpr (FINE == 1) STAMP(20);
    wait_all<8>();
    cs_read(IC<24>{});
    bar();
    if constexpr (FINE == 1) STAMP(47);
    static_for<4 * 28, 4 * 28 + 2>(piece);
    cs_add(csb);
    cs_read(IC<25>{});
    static_for<4 * 28 + 2, 4 * 29 + 1>(piece);
    cs_add(csb);
    cs_read(IC<26>{});
    static_for<4 * 29 + 1, 4 * 30>(piece);
    cs_add(csb);
    put_e(true, lane, csb[0]);
    if constexpr (FINE == 1) STAMP(23);
    wait_bar<0>();
    if constexpr (FINE == 1) STAMP(50);
    put_flag(srd_ws, FL_0 + b * 16 + (s & 1) * 8, tag, s < 2 ? 8 : 0);
    static_for<0, 2>([&](auto J_) {
      f32x4 cg = {};
      cs_read(IC<(J_ ? 27 : 23)>{}); cs_add(cg);
      const u32x2 gr = {((__float_as_uint(cg[0]) + 0x80u) & 0xffffff00u) | ((unsigned)tag & 0xffu), (unsigned)(tag >> 8)};
      st8c<0>(gr, (!J_ || gap) ? lane * 8 : OOB, srd_ws, so_cs + kst(J_) * 512);
    });
    __builtin_amdgcn_s_sleep(EPI_NAP);
    static_for<4 * 30, 4 * 34>(piece);
    wait_vm<8>(); full(1);
    if constexpr (FINE == 1) STAMP(25);

    u32x4 br[60], ar[48];
    const unsigned so_b = WS_R + jn * R_J + s * 5120;
    unsigned sob, vb_early;
    auto bload = [&](auto I_) {
      constexpr int i = I_;
      if constexpr (i % 5 == 0) { sob = so_b + (i / 5) * 20480; PINS(sob); }
      if constexpr (i % 5 == 4) { sob += 4096; PINS(sob); }
      ld16c<(i % 5 < 4 ? (i % 5) * 1024 : 0)>(br[i], i < NB0 ? vb_early : l16, srd_ws, sob);
    };
    const unsigned so_a = WS_A2 + mt * A2_MT + s * 4096;
    unsigned aa = (lane >> 4) * 1024 + (lane & 15) * 16;
    PIN(aa);
    unsigned soa, va_early;
    auto aload = [&](auto I_) {
      constexpr int i = I_;
      if constexpr ((i & 3) == 0) { soa = so_a + (i >> 2) * 16384; PINS(soa); }
      ld16c<(i & 3) * 256>(ar[i], i < NAE ? va_early : aa, srd_ws, soa);
    };
    {
      u32x4 f0;
      const int f0r = lane >> 1;
      const unsigned vf0 = (((10 * (f0r < 12 ? 4 * f0r + s : f0r + 36) + ((80 * jn + 79 * (lane & 1)) >> 6)) * 683) >> 11) * 16;
      u32x4 cg[5], f1;
      auto cs_load = [&]() INL {
        ld16c<0>(cg[0], l16, srd_ws, so_cs); ld16c<1024>(cg[1], l16, srd_ws, so_cs); ld16c<2048>(cg[2], l16, srd_ws, so_cs); ld16c<3072>(cg[3], l16, srd_ws, so_cs);
        ld16c<0>(cg[4], l16, srd_ws, so_cs + 4096);
      };
      await<15, SY_DONE, NAPS>(dn, sy);
      static_for<4 * 34, 4 * 36>(piece);
      ld16c<0>(f0, vf0, srd_ws, xcd * FL_REP + FL_0);
      cs_load();
      wait_vm<6>(); full(2);
      wait_vm<5>();
      PIN(f0);
      const bool early = __all(tagged(f0, tag) || 5 * f0r >= NB0);
      vb_early = early ? l16 : OOB;
      STAMP(26);
      const int tq = 4 * qb + s, sel3 = lane >> 4;
      const unsigned vf1 = (sel3 == 0 ? (qb ? tq - 4 : tq) : (sel3 == 1 ? tq : mt)) * 256 + (l16 & 255);
      static_for<0, NB0 - NBF>(bload);
      ld16c<0>(f1, vf1, srd_ws, xcd * FL_REP + FL_1);
      static_for<NB0 - NBF, NB0>(bload);
      wait_vm<NB0 + 1>();
      PIN(cg[0]); PIN(cg[1]); PIN(cg[2]); PIN(cg[3]); PIN(cg[4]);
      __builtin_amdgcn_s_setprio(3);
#pragma unroll 1
      for (unsigned spin = 0; spin < SPIN_CAP; ++spin) {
        unsigned bad = 0;
#pragma unroll
        for (int j = 0; j < 5; ++j)
          bad |= (((cg[j][0] ^ (unsigned)tag) | (cg[j][2] ^ (unsigned)tag)) & 0xffu) | (cg[j][1] ^ (unsigned)(tag >> 8)) | (cg[j][3] ^ (unsigned)(tag >> 8));
        if (__all(bad == 0u)) break;
        __builtin_amdgcn_s_sleep(POLL_SLEEP);
        cs_load();
        wait_vm<0>();
        PIN(cg[0]); PIN(cg[1]); PIN(cg[2]); PIN(cg[3]); PIN(cg[4]);
      }
      {
        float v0 = 0.f, v1 = 0.f;
#pragma unroll
        for (int j = 0; j < 5; ++j) { v0 += __uint_as_float(cg[j][0] & 0xffffff00u); v1 += __uint_as_float(cg[j][2] & 0xffffff00u); }
        put_e(false, 2 * l31 + hh, swap_add32(v0, v1));
        event(1);
      }
      unsigned cm_kv = 0u, cm_q = 0u;
#pragma unroll
      for (int j = 0; j < 6; ++j) {
        const int u = 10 * (j >> 1) + ((o0 + 2 * (j & 1)) >> 3), cu = u < 24 ? 2 * ((u * 11) >> 5) : 2 * perm6(u - 24) + 1;
        if (j < 2) cm_q |= 1u << cu; else cm_kv |= 1u << cu;
      }
      const bool need_a = (((sel3 == 0 ? cm_kv : (sel3 == 1 ? cm_kv | cm_q : 0u)) >> (lane & 15)) & 1u) != 0u, need_h = sel3 >= 2 && (lane & 1);
      wait_vm<NBF>();
      PIN(f1);
      STAMP(27);
      if (!__all(tagged(f1, tag) || !need_a)) f1 = poll(srd_ws, vf1, xcd * FL_REP + FL_1, tag, need_a);
      const bool early_h = __all(tagged(f1, tag) || !need_h);
      const unsigned s1o = WS_QKV + (4 * qb + s) * QKV_TB + o0 * 1024, s0o = qb ? s1o - 4 * QKV_TB : s1o;
      await<17, SY_DONE, NAPS>(dn, sy);
      static_for<0, 3>([&](auto J_) {
        constexpr int i = J_;
        dma16c<L_Q + i * 4096, i * 1024>(d_s, l16, srd_ws, s1o);
        dma16c<krow(i) + 4096, (80 + i) * 1024>(d_s, l16, srd_ws, s1o);
      });
      STAMP(1);
      await<19, SY_DONE, NAPS>(dn, sy);
      __builtin_amdgcn_s_setprio(2);
#pragma unroll
      for (int hb = 0; hb < 2; ++hb)
        *(LAS u32x4*)(L0 + L_FR + (256 * hb + 64 * s) * 16 + l16) = (u32x4){(qb == 0 && hb == 0) ? 0x3f803f80u : 0x3f800000u, 0x3f803f80u, 0x3f803f80u, 0x3f803f80u};
      static_for<0, 3>([&](auto J_) { constexpr int i = J_; dma16c<(3 + i) * AROW + 4096, (160 + i) * 1024>(d_s, l16, srd_ws, s1o); });
      static_for<0, 3>([&](auto J_) {
        constexpr int i = J_;
        dma16c<krow(i), (80 + i) * 1024>(d_s, l16, srd_ws, s0o);
        dma16c<(3 + i) * AROW, (160 + i) * 1024>(d_s, l16, srd_ws, s0o);
      });
      ld16c<0>(f1, vf1, srd_ws, xcd * FL_REP + FL_1);
      ld16c<0>(f0, vf0, srd_ws, xcd * FL_REP + FL_0);
      __builtin_amdgcn_s_setprio(1);
      va_early = early_h ? aa : OOB;
      static_for<0, NAE>(aload);
      __builtin_amdgcn_s_setprio(3);
      wait_vm<11 + NAE>();
      STAMP(53);
      const unsigned aq = d_s + L_Q + l31 * 16, ak = d_s + 4096 + l31 * 16;
      u32x4 q0[2], q1[2];
      f32x16 t[2];
#pragma unroll
      for (int a = 0; a < 2; ++a) {
        q0[a] = *(const LAS u32x4*)(aq + hh * 4096 + a * 512); q1[a] = *(const LAS u32x4*)(aq + 8192 + a * 512);
        const bool z0 = par && !hh;
        q0[a][0] = z0 ? 0u : q0[a][0]; q0[a][1] = z0 ? 0u : q0[a][1];
        q1[a][0] = hh ? 0u : q1[a][0]; q1[a][1] = hh ? 0u : q1[a][1];
        q1[a][2] = z0 ? q1[a][2] : 0u; q1[a][3] = z0 ? q1[a][3] : 0u;
        t[a] = mfma32(lds_b128(ak + hh * AROW + a * 512), asbf(q0[a]), (f32x16){});
        t[a] = mfma32(lds_b128(ak + L_K2 + a * 512), asbf(q1[a]), t[a]);
        *(LAS u32x2*)(aq + hh * 4096 + a * 512) = (u32x2){q0[a][0], q0[a][1]};
      }
#pragma unroll
      for (int a = 0; a < 2; ++a) {
        float mx = __builtin_amdgcn_fmed3f(t[a][0], t[a][1], __builtin_inff()), my = max3(mx, t[a][2], t[a][3]);
#pragma unroll
        for (int r = 4; r < 16; r += 4) { mx = max3(mx, t[a][r], t[a][r + 1]); my = max3(my, t[a][r + 2], t[a][r + 3]); }
        const unsigned ref = pk2(-1e30f, -swap_max32(max3(mx, my, my)));
        q1[a][0] = hh ? ref : q1[a][0];
        *(LAS u32x4*)(aq + 8192 + hh * 4096 + a * 512) = q1[a];
      }
      SBAR();
      STAMP(54);
      wait_vm<2 + NAE>();
      event(2);
      __builtin_amdgcn_s_setprio(0);
      STAMP(2);
      wait_vm<NAE>();
      PIN(f1); PIN(f0);
      if (!__all(tagged(f0, tag))) poll(srd_ws, vf0, xcd * FL_REP + FL_0, tag);
      if (!(early && early_h)) {
        if (!__all(tagged(f1, tag) || !need_h)) poll(srd_ws, vf1, xcd * FL_REP + FL_1, tag, need_h);
        wait_vm<0>();
        const unsigned va = early_h ? OOB : aa, vb = early ? OOB : l16;
        if (!early_h) static_for<0, NAE>([&](auto I_) { reld16c<(I_ & 3) * 256>(ar[I_], va, srd_ws, so_a + (I_ >> 2) * 16384); });
        if (!early) static_for<0, NB0>([&](auto I_) { reld16c<0>(br[I_], vb, srd_ws, so_b + (I_ / 5) * 20480 + (I_ % 5) * 1024); });
        wait_vm<0>();
      }
      static_for<NAE, NA0>(aload);
    }
    STAMP(28);
    await<24, SY_DONE, NAPS>(dn, sy);
    {
      f32x4 lo, hi;
#pragma unroll
      for (int j = 0; j < 4; ++j) { const bool far = j + 8 * s + 4 * hh > l31; lo[j] = far ? 0.f : -1e30f; hi[j] = far ? -1e30f : 0.f; }
      *(LAS f32x4*)(d_s + L_MK + l16) = lo;
      *(LAS f32x4*)(d_s + L_MK + 4096 + l16) = hi;
      event(3);
    }
    STAMP(3);
    {
      f32x4 acc[4][5];
      u32x4 f2;
      const unsigned so_w = WS_R + jn * R_J + 48 * 5120 + s * 1024;
      struct Plan { int n, op[160], end[13]; };
      constexpr Plan pl = [] {
        Plan p{}; int a = NA0, bq = NB0, wv = 0;
        auto A = [&](int e) { while (a < e) p.op[p.n++] = 100 + a++; };
        auto B = [&](int e) { while (bq < e) p.op[p.n++] = 200 + bq++; };
        auto W = [&](int e) { while (wv < e) p.op[p.n++] = 300 + wv++; };
        A(12); B(20); A(16); B(25); W(4); p.end[0] = p.n;
        B(32); W(9); p.end[1] = p.n;
        B(39); W(14); p.end[2] = p.n;
        B(NBH); W(18 - NWX); p.end[3] = p.n;
        for (int q = 4; q < 12; ++q) { if (q > 4) { A(imin(48, 4 * q + 16)); B(imin(60, NBH + 5 * (q - 4))); } else a = 32; p.end[q] = p.n; }
        return p;
      }();
      auto issue = [&](auto C_) {
        constexpr int cd = C_;
        if constexpr (cd < 200) aload(IC<cd - 100>{});
        else if constexpr (cd < 300) bload(IC<cd - 200>{});
        else dma16c<L_W0 + (cd - 300) * 4096, (cd - 300) * 4096>(d_s, l16, srd_ws, so_w);
        SBAR();
      };
      constexpr auto young = [pl](int cd, int q) constexpr { for (int i = 0; i < pl.n; ++i) if (pl.op[i] == cd) return pl.end[q - 1] - 1 - i; return 63; };
      auto step = [&](auto Q_) {
        constexpr int q = Q_, o0 = q ? pl.end[q - 1] : 0, no = pl.end[q] - o0;
        static_for<0, 20>([&](auto M_) {
          constexpr int m = M_, i = m / 5, t = m % 5;
          mma16<q == 0>(acc[i][t], asbf(br[5 * q + t]), asbf(ar[4 * q + i]));
          SBAR();
          static_for<o0 + m * no / 20, o0 + (m + 1) * no / 20>([&](auto J_) { issue(IC<pl.op[J_]>{}); });
        });
      };
      static_assert(NA0 >= 8 && NA0 <= 16 && NB0 >= 10 && NB0 <= 25, "early loads");
      STAMP(4);
      wait_vm<NA0 - 4>();
      STAMP(5);
      step(IC<0>{});
      static_for<1, 4>([&](auto Q_) {
        constexpr int q = Q_, ya = 4 * q + 3 < NA0 ? NA0 - 4 * q - 4 + pl.end[q - 1] : young(100 + 4 * q + 3, q);
        wait_vm<imin(ya, young(200 + 5 * q + 4, q))>();
        step(Q_);
      });
      ld16c<0>(f2, l16 & 255, srd_ws, jn * FL_REP + FL_2 + mt * 256);
      static_for<18 - NWX, 18>([&](auto J_) { issue(IC<300 + J_>{}); });
      STAMP(6);
      wait_vm<NWX>();
      PIN(f2);
      if (!__all(tagged(f2, tag))) poll(srd_ws, l16 & 255, jn * FL_REP + FL_2 + mt * 256, tag);
      STAMP(55);
      static_for<16, 32>(aload);
      wait_vm<16>();
      event(4);
      STAMP(56);
      static_for<4, 12>([&](auto Q_) {
        constexpr int q = Q_, ya = q < 8 ? 4 * (7 - q) + pl.end[q - 1] - pl.end[3] : young(100 + 4 * q + 3, q);
        constexpr int yb = 5 * q + 4 < NBH ? 63 : young(200 + 5 * q + 4, q);
        if constexpr (FINE == 2 || (FINE == 5 && (q == 8 || q == 11))) STAMP(8 + q);
        if constexpr (q == QUP) __builtin_amdgcn_s_setprio(2);
        wait_vm<imin(ya, yb)>();
        step(Q_);
      });
      STAMP(57);
      unsigned ap = L0 + L_P0 + s * 5120 + l16, ap3 = L0 + L_P3 + s * 5120 + l16;
      PIN(ap); PIN(ap3);
      await<26, SY_DONE, 1>(dn, sy);
      mma_settle();
      SBAR();
#pragma unroll
      for (int i = 0; i < 4; ++i)
#pragma unroll
        for (int t = 0; t < 5; ++t) *(LAS f32x4*)((i < 3 ? ap + i * 20480 : ap3) + t * 1024) = acc[i][t];
      event(5);
      STAMP(7);
      if (b == 0 && s == 0) {
        const unsigned lv = fresh_lane() * 16;
#pragma unroll 1
        for (int j = 0; j < 4; ++j) poll(srd_ws, lv, FL_0 + j * 1024, tag);
        put_flag(srd_ws, FL_EPOCH, tag, 1);
      }
    }
  }
  asm volatile(".Lcode_end:");
#endif
}

extern "C" void kernel_launch(void* const* d_in, const int* in_sizes, int n_in, void* d_out, int out_size, void* d_ws, size_t ws_size,
                              hipStream_t stream) {
  const unsigned mode = 0x00020000u;
  Params P;
  P.x = {d_in[0], SEQ * DM * 2, mode};
  P.wqkv = {d_in[1], DM * 3 * DM * 2, mode};
  P.wo = {d_in[2], DM * DM * 2, mode};
  P.wg = {d_in[4], DM * FF * 2, mode};
  P.wu = {d_in[5], DM * FF * 2, mode};
  P.wd = {d_in[6], FF * DM * 2, mode};
  P.out = {d_out, SEQ * DM * 2, mode};
  P.ws = {d_ws, WS_STAMP, mode};
  fused<<<256, 512, 0, stream>>>(P);
}
```
